```python
import torch

def _softmax(x, axis=-1):
    return torch.softmax(x, dim=axis)
def _gelu(x):
    return torch.nn.functional.gelu(x, approximate="tanh")
def _silu(x):
    return torch.nn.functional.silu(x)
def _sigmoid(x):
    return torch.sigmoid(x)
def _layernorm(x, axis=-1, eps=1e-5):
    m = x.mean(dim=axis, keepdim=True)
    v = x.var(dim=axis, keepdim=True, unbiased=False)
    return (x - m) / torch.sqrt(v + eps)
def _scatter_add(tgt, idx, src, axis=0):
    return tgt.clone().index_add_(axis, idx, src)
def _segment_sum(src, seg, n):
    out = torch.zeros((n,) + src.shape[1:], dtype=src.dtype)
    return out.index_add_(0, seg, src)
def _pad(x, pw):
    flat = tuple(v for pair in reversed(pw) for v in pair)
    return torch.nn.functional.pad(x, flat)

kernel_name = 'sequential_preLN__ssm_conv_h3__glu_gated_geglu__prompt'

def setup_inputs():

    _w = (lambda a: (lambda t: t.to(torch.bfloat16) if t.is_floating_point() else t)(torch.as_tensor(a)))
    def _A_setup():
        B, S, D = 1, 2048, 640
        ks = 4
        return {
            "q": _w((lambda *s: torch.randn(*s, dtype=torch.bfloat16) * 0.05)(B, S, D)),
            "k": _w((lambda *s: torch.randn(*s, dtype=torch.bfloat16) * 0.2)(B, S, D)),
            "v": _w((lambda *s: torch.randn(*s, dtype=torch.bfloat16) * 0.2)(B, S, D)),
            "shift_k": _w((lambda *s: torch.randn(*s, dtype=torch.bfloat16) * 0.2)(D, ks) * 0.5),
            "ssm_k": _w((lambda *s: torch.randn(*s, dtype=torch.bfloat16) * 0.4)(D, S) * (1.0 / S ** 0.5)),
            "ssm_D": _w((lambda *s: torch.randn(*s, dtype=torch.bfloat16) * 0.2)(D)),
        }
    _A_in = _A_setup()

    def _B_setup():
        B, S, Dm, Df = 1, 2048, 640, 1536
        return {
            "x": _w((lambda *s: torch.randn(*s, dtype=torch.bfloat16) * 0.05)(B, S, Dm)),
            "w_gate": _w((lambda *s: torch.randn(*s, dtype=torch.bfloat16) * 0.4)(Dm, Df) * (Dm ** -0.5)),
            "w_up": _w((lambda *s: torch.randn(*s, dtype=torch.bfloat16) * 0.4)(Dm, Df) * (Dm ** -0.5)),
            "w_down": _w((lambda *s: torch.randn(*s, dtype=torch.bfloat16) * 0.4)(Df, Dm) * (Df ** -0.5)),
        }
    _B_in = _B_setup()

    return {
        "A_q": _A_in["q"],
        "A_k": _A_in["k"],
        "A_v": _A_in["v"],
        "A_shift_k": _A_in["shift_k"],
        "A_ssm_k": _A_in["ssm_k"],
        "A_ssm_D": _A_in["ssm_D"],
        "B_x": _B_in["x"],
        "B_w_gate": _B_in["w_gate"],
        "B_w_up": _B_in["w_up"],
        "B_w_down": _B_in["w_down"],
    }

def reference(A_q, A_k, A_v, A_shift_k, A_ssm_k, A_ssm_D, B_x, B_w_gate, B_w_up, B_w_down):
    def _A_ref(q, k, v, shift_k, ssm_k, ssm_D):
        B, S, D = q.shape
        ks = shift_k.shape[-1]
        k_pad = _pad(k, ((0, 0), (ks - 1, 0), (0, 0)))
        k_shift = torch.zeros_like(k)
        for t in range(ks):
            k_shift = k_shift + k_pad[:, t:t + S, :] * shift_k[:, ks - 1 - t][None, None, :]
        kv = k_shift * v
        L = 2 * S
        kv_pad = _pad(kv, ((0, 0), (0, S), (0, 0)))
        kf_pad = _pad(ssm_k, ((0, 0), (0, S)))
        kv_f = (lambda x, axes=(-2,-1): torch.fft.rfft2(x.float(), dim=axes))(kv_pad[:, None, :, :], axes=(1, 2))
        kf_f = (lambda x, axes=(-2,-1): torch.fft.rfft2(x.float(), dim=axes))(kf_pad[:, None, :], axes=(1, 2))
        kf_f = (lambda x, *d: x.permute(*d))(kf_f, 1, 2, 0)
        y_f = kv_f * kf_f[None, :, :, :]
        y = (lambda x, s, axes=(-2,-1): torch.fft.irfft2(x, s=s, dim=axes).to(torch.bfloat16))(y_f, (1, L), axes=(1, 2))[:, 0, :S, :]
        y = y + kv * ssm_D[None, None, :]
        return q * y

    def _B_ref(x, w_gate, w_up, w_down):
        g = torch.einsum("bsd,df->bsf", x, w_gate)
        u = torch.einsum("bsd,df->bsf", x, w_up)
        h = _gelu(g) * u
        return torch.einsum("bsf,fd->bsd", h, w_down)

    x = A_q
    h = x + _A_ref(_layernorm(x, axis=-1), A_k, A_v, A_shift_k, A_ssm_k, A_ssm_D)
    h2 = h + _B_ref(_layernorm(h, axis=-1), B_w_gate, B_w_up, B_w_down)
    return h2
```

```cpp
#include <hip/hip_runtime.h>
#include <hip/hip_bf16.h>
#include <stdint.h>

constexpr int S = 2048, D = 640, F = 1536;
typedef unsigned short u16;
__device__ __forceinline__ float bf2f(u16 b) { return __uint_as_float(((unsigned)b) << 16); }
__device__ __forceinline__ u16 f2bf(float f) { __hip_bfloat16 h = __float2bfloat16(f); return *(u16*)&h; }
__device__ __forceinline__ float rbf(float f) { return bf2f(f2bf(f)); }

__global__ void k_kv(const u16* k, const u16* v, const u16* sk, u16* kvT) {
  int idx = blockIdx.x * 256 + threadIdx.x;
  int s = idx / D, d = idx % D;
  float acc = 0.f;
  for (int t = 0; t < 4; ++t) {
    int ss = s + t - 3;
    float kk = ss >= 0 ? bf2f(k[ss * D + d]) : 0.f;
    acc = rbf(acc + rbf(kk * bf2f(sk[d * 4 + 3 - t])));
  }
  kvT[d * S + s] = f2bf(acc * bf2f(v[idx]));
}
__global__ void k_conv(const u16* kvT, const u16* hk, const u16* Dv, u16* yp) {
  int d = blockIdx.y;
  int s = blockIdx.x * 256 + threadIdx.x;
  float acc = 0.f;
  for (int j = 0; j <= s; ++j) acc += bf2f(hk[d * S + j]) * bf2f(kvT[d * S + s - j]);
  float y = rbf(acc);
  y = rbf(y + rbf(bf2f(kvT[d * S + s]) * bf2f(Dv[d])));
  yp[s * D + d] = f2bf(y);
}
__device__ float block_sum(float v, float* sm) {
  for (int o = 32; o > 0; o >>= 1) v += __shfl_xor(v, o);
  __syncthreads();
  if ((threadIdx.x & 63) == 0) sm[threadIdx.x >> 6] = v;
  __syncthreads();
  float r = 0.f;
  for (int i = 0; i < (int)(blockDim.x >> 6); ++i) r += sm[i];
  return r;
}
__global__ void k_ln(const u16* x, const u16* yp, u16* h, u16* hn) {
  __shared__ float sm[16];
  int s = blockIdx.x, d = threadIdx.x;
  float xv = bf2f(x[s * D + d]);
  float m = block_sum(xv, sm) / D;
  float var = block_sum((xv - m) * (xv - m), sm) / D;
  float sd = rbf(sqrtf(rbf(rbf(var) + 1e-5f)));
  float q = rbf(rbf(xv - rbf(m)) / sd);
  float hv = rbf(xv + rbf(q * bf2f(yp[s * D + d])));
  h[s * D + d] = f2bf(hv);
  float m2 = block_sum(hv, sm) / D;
  float var2 = block_sum((hv - m2) * (hv - m2), sm) / D;
  float sd2 = rbf(sqrtf(rbf(rbf(var2) + 1e-5f)));
  hn[s * D + d] = f2bf(rbf(hv - rbf(m2)) / sd2);
}
__global__ void k_g1(const u16* hn, const u16* wg, const u16* wu, u16* hh) {
  int f = blockIdx.x * 256 + threadIdx.x;
  int s = blockIdx.y;
  float g = 0.f, u = 0.f;
  for (int d = 0; d < D; ++d) {
    float a = bf2f(hn[s * D + d]);
    g += a * bf2f(wg[d * F + f]);
    u += a * bf2f(wu[d * F + f]);
  }
  g = rbf(g); u = rbf(u);
  float t = tanhf(0.7978845608028654f * (g + 0.044715f * g * g * g));
  float ge = rbf(0.5f * g * (1.f + t));
  hh[s * F + f] = f2bf(ge * u);
}
__global__ void k_g2(const u16* hh, const u16* wd, const u16* h, u16* out) {
  int d = blockIdx.x * 128 + threadIdx.x;
  int s = blockIdx.y;
  float o = 0.f;
  for (int f = 0; f < F; ++f) o += bf2f(hh[s * F + f]) * bf2f(wd[f * D + d]);
  out[s * D + d] = f2bf(bf2f(h[s * D + d]) + rbf(o));
}

extern "C" void kernel_launch(void* const* d_in, const int* in_sizes, int n_in,
                              void* d_out, int out_size, void* d_ws, size_t ws_size,
                              hipStream_t stream) {
  const u16* q = (const u16*)d_in[0];
  const u16* k = (const u16*)d_in[1];
  const u16* v = (const u16*)d_in[2];
  const u16* sk = (const u16*)d_in[3];
  const u16* hk = (const u16*)d_in[4];
  const u16* Dv = (const u16*)d_in[5];
  const u16* wg = (const u16*)d_in[7];
  const u16* wu = (const u16*)d_in[8];
  const u16* wd = (const u16*)d_in[9];
  u16* out = (u16*)d_out;
  u16* ws = (u16*)d_ws;
  u16* kvT = ws;
  u16* yp = kvT + S * D;
  u16* h = yp + S * D;
  u16* hn = h + S * D;
  u16* hh = hn + S * D;
  k_kv<<<S * D / 256, 256, 0, stream>>>(k, v, sk, kvT);
  k_conv<<<dim3(S / 256, D), 256, 0, stream>>>(kvT, hk, Dv, yp);
  k_ln<<<S, D, 0, stream>>>(q, yp, h, hn);
  k_g1<<<dim3(F / 256, S), 256, 0, stream>>>(hn, wg, wu, hh);
  k_g2<<<dim3(D / 128, S), 128, 0, stream>>>(hh, wd, h, out);
}
```

```cpp
#include <hip/hip_runtime.h>
#include <stdint.h>

namespace {
struct Desc { const void* p; uint32_t n; uint32_t f; };
struct Args { Desc d[12]; };

__global__ __launch_bounds__(256, 1) void k_block(Args args) {
  __shared__ __attribute__((aligned(16))) char smem[163840];
  asm volatile(
    "s_load_dwordx16 s[16:31], s[0:1], 0\n"
    "s_load_dwordx16 s[48:63], s[0:1], 128\n"
    "s_load_dwordx16 s[32:47], s[0:1], 64\n"
    "s_getpc_b64 s[4:5]\n"
    ".PC_%=:\n"
    "s_mov_b32 s6, .END_%=-.PC_%=\n"
    "s_mov_b32 s7, 0x20000\n"
    "s_bfe_u32 s8, s2, 0x50003\n"
    "s_lshl_b32 s8, s8, 11\n"
    "v_lshrrev_b32 v2, 4, v0\n"
    "v_lshlrev_b32 v2, 7, v2\n"
    "buffer_load_dword v255, v2, s[4:7], s8 offen\n"
    "v_and_b32 v1, 63, v0\n"
    "s_cmp_ge_u32 s2, 160\n"
    "s_cbranch_scc1 .SP_%=\n"
    ".CV_%=:\n"
    "s_and_b32 s6, s2, 15\n"
    "s_lshr_b32 s7, s2, 4\n"
    "s_mul_i32 s8, s6, 163840\n"
    "s_lshl_b32 s98, s7, 7\n"
    "s_add_u32 s8, s8, s98\n"
    "v_lshrrev_b32 v19, 3, v0\n"
    "v_and_b32 v20, 7, v0\n"
    "v_mul_u32_u24 v248, 5120, v19\n"
    "v_lshl_add_u32 v248, v20, 4, v248\n"
    "v_add_u32 v248, s8, v248\n"
    "v_subrev_u32 v249, 3840, v248\n"
    "v_lshlrev_b32 v22, 3, v1\n"
    "v_lshrrev_b32 v2, 6, v0\n"
    "s_nop 0\n"
    "v_readfirstlane_b32 s3, v2\n"
    "s_lshl_b32 s66, s7, 9\n"
    "s_lshl2_add_u32 s8, s2, s3\n"
    "s_lshl_b32 s67, s8, 12\n"
    "s_lshl_b32 s13, s8, 1\n"
    "s_mul_i32 s7, s7, 526336\n"
    "s_lshl2_add_u32 s6, s6, s3\n"
    "s_lshl_b32 s6, s6, 8\n"
    "s_add_u32 s6, s6, s7\n"
    "v_and_b32 v16, 15, v1\n"
    "v_lshrrev_b32 v17, 4, v1\n"
    "v_mul_u32_u24 v21, 32896, v17\n"
    "v_lshl_add_u32 v21, v16, 4, v21\n"
    "v_add_u32 v21, s6, v21\n"
    "s_and_b32 s8, s2, 3\n"
    "s_lshl_b32 s4, s2, 7\n"
    "s_add_u32 s4, s4, 36864\n"
    "s_bfe_u32 s12, s2, 0x20003\n"
    "s_lshl2_add_u32 s12, s8, s12\n"
    "s_bfe_u32 s8, s2, 0x10002\n"
    "s_lshr_b32 s11, s2, 5\n"
    "s_lshl3_add_u32 s11, s8, s11\n"
    "s_lshl4_add_u32 s5, s11, s12\n"
    "s_lshl_b32 s5, s5, 7\n"
    "s_add_u32 s5, s5, 69632\n"
    "s_lshr_b32 s14, s3, 1\n"
    "s_mul_i32 s8, s3, 16512\n"
    "v_add_u32 v18, s8, v22\n"
    "v_lshlrev_b32 v2, 4, v1\n"
    "v_add_u32 v250, s8, v2\n"
    "v_add_u32 v251, 10240, v18\n"
    "v_mov_b32 v208, 0\n"
    "v_mov_b32 v209, 0\n"
    "v_mov_b32 v210, 0\n"
    "v_mov_b32 v211, 0\n"
    "s_mov_b64 exec, 0xff\n"
    "ds_write_b128 v250, v[208:211] offset:6272\n"
    "ds_write_b128 v250, v[208:211] offset:7424\n"
    "ds_write_b128 v250, v[208:211] offset:8576\n"
    "ds_write_b128 v250, v[208:211] offset:14336\n"
    "s_mov_b64 exec, -1\n"
    "v_lshl_add_u32 v23, v20, 6, s8\n"
    "v_mul_u32_u24 v20, 528, v20\n"
    "v_lshl_add_u32 v20, v17, 6, v20\n"
    "v_and_b32 v19, 1, v19\n"
    "v_xor_b32 v19, 1, v19\n"
    "v_lshl_add_u32 v19, v19, 3, v20\n"
    "v_add_u32 v19, s8, v19\n"
    "v_lshrrev_b32 v20, 5, v1\n"
    "v_lshl_add_u32 v20, v20, 4, s8\n"
    "v_lshl_add_u32 v20, v1, 4, v20\n"
    "v_lshl_add_u32 v28, v17, 3, v16\n"
    "s_add_u32 s6, s8, 8\n"
    "v_lshl_add_u32 v11, v28, 3, s6\n"
    "s_mul_i32 s8, s3, 4640\n"
    "s_add_u32 s8, s8, 66048\n"
    "v_add_u32 v28, s8, v22\n"
    "v_mov_b32 v30, 0\n"
    "v_mov_b32 v31, 0\n"
    "ds_write_b64 v28, v[30:31]\n"
    "v_lshlrev_b32 v28, 1, v16\n"
    "v_sub_u32 v28, v28, v17\n"
    "s_add_u32 s8, s8, 80\n"
    "v_lshl_add_u32 v12, v28, 4, s8\n"
    "s_mul_i32 s8, s3, 1152\n"
    "s_lshl_b32 s6, s14, 5\n"
    "s_add_u32 s8, s8, s6\n"
    "v_lshl_add_u32 v28, v16, 2, v17\n"
    "v_lshl_add_u32 v13, v28, 3, s8\n"
    "v_lshrrev_b32 v28, 2, v16\n"
    "v_mul_u32_u24 v29, 1152, v28\n"
    "v_lshrrev_b32 v28, 1, v28\n"
    "v_lshl_add_u32 v29, v28, 5, v29\n"
    "v_and_b32 v28, 3, v16\n"
    "v_lshl_add_u32 v29, v28, 3, v29\n"
    "v_lshrrev_b32 v30, 4, v0\n"
    "v_lshl_add_u32 v14, v30, 6, v29\n"
    "v_mul_u32_u24 v15, 163840, v30\n"
    "v_lshl_add_u32 v15, v16, 4, v15\n"
    "v_mul_u32_u24 v97, 4640, v28\n"
    "v_lshrrev_b32 v29, 2, v0\n"
    "v_lshl_add_u32 v97, v29, 4, v97\n"
    "v_add_u32 v97, 66560, v97\n"
    "s_andn2_b32 s6, s2, 15\n"
    "s_add_u32 s6, s6, s3\n"
    "s_lshl_b32 s6, s6, 7\n"
    "s_add_u32 s6, s6, 4096\n"
    "v_lshl_add_u32 v98, v28, 9, s6\n"
    "s_lshl_b32 s6, s3, 9\n"
    "s_add_u32 s6, s6, 24576\n"
    "v_lshl_add_u32 v29, v28, 7, s6\n"
    "v_lshrrev_b32 v28, 2, v16\n"
    "v_lshl_add_u32 v29, v28, 11, v29\n"
    "v_cmp_gt_u32 vcc, 32, v1\n"
    "s_nop 1\n"
    "v_cndmask_b32 v98, v29, v98, vcc\n"
    "s_and_b32 s8, s2, 3\n"
    "s_lshl_b32 s8, s8, 7\n"
    "s_add_u32 s8, s8, 32768\n"
    "v_and_b32 v29, 7, v1\n"
    "v_lshl_add_u32 v29, v29, 9, s8\n"
    "s_and_b32 s6, s2, 16\n"
    "s_lshl_b32 s6, s6, 5\n"
    "s_add_u32 s8, s8, s6\n"
    "s_sub_u32 s8, s8, 8192\n"
    "v_min_u32 v3, 11, v1\n"
    "v_lshl_add_u32 v3, v3, 10, s8\n"
    "v_cmp_gt_u32 vcc, 48, v1\n"
    "s_nop 1\n"
    "v_cndmask_b32 v98, v29, v98, vcc\n"
    "s_waitcnt lgkmcnt(0)\n"
    "s_mul_i32 s61, s3, 16512\n"
    "v_min_u32 v28, 5, v1\n"
    "v_lshrrev_b32 v29, 1, v28\n"
    "v_and_b32 v28, 1, v28\n"
    "v_lshlrev_b32 v29, 17, v29\n"
    "v_lshl_add_u32 v28, v28, 15, v29\n"
    "s_sub_u32 s8, s4, 32768\n"
    "buffer_load_dword v30, v28, s[52:55], s8 offen sc1\n"
    "buffer_load_dword v31, v29, s[52:55], s5 offen sc1\n"
    "buffer_load_dwordx2 v[60:61], v22, s[28:31], s66 offen\n"
    "s_add_u32 m0, s61, 5248\n"
    "s_nop 0\n"
    "buffer_load_dwordx4 v248, s[20:23], 0 offen offset:0 nt lds\n"
    "s_add_u32 m0, s61, 5120\n"
    "s_nop 0\n"
    "buffer_load_dwordx4 v248, s[20:23], 0 offen offset:1280 nt lds\n"
    "s_add_u32 m0, s61, 4992\n"
    "s_nop 0\n"
    "buffer_load_dwordx4 v248, s[20:23], 0 offen offset:2560 nt lds\n"
    "s_add_u32 m0, s61, 4864\n"
    "s_nop 0\n"
    "buffer_load_dwordx4 v248, s[20:23], 0 offen offset:3840 nt lds\n"
    "s_mov_b64 exec, 0xff\n"
    "s_add_u32 m0, s61, 6272\n"
    "s_nop 0\n"
    "buffer_load_dwordx4 v249, s[20:23], 0 offen offset:0 nt lds\n"
    "s_add_u32 m0, s61, 6144\n"
    "s_nop 0\n"
    "buffer_load_dwordx4 v249, s[20:23], 0 offen offset:1280 nt lds\n"
    "s_add_u32 m0, s61, 6016\n"
    "s_nop 0\n"
    "buffer_load_dwordx4 v249, s[20:23], 0 offen offset:2560 nt lds\n"
    "s_mov_b64 exec, -1\n"
    "buffer_load_dwordx4 v[64:67], v248, s[24:27], 0 offen offset:0 nt\n"
    "buffer_load_dwordx4 v[68:71], v248, s[24:27], 0 offen offset:1280 nt\n"
    "buffer_load_dwordx4 v[72:75], v248, s[24:27], 0 offen offset:2560 nt\n"
    "buffer_load_dwordx4 v[76:79], v248, s[24:27], 0 offen offset:3840 nt\n"
    "buffer_load_dwordx2 v[24:25], off, s[52:55], 0 sc1\n"
    "v_and_b32 v152, 15, v1\n"
    "v_lshrrev_b32 v153, 4, v1\n"
    "v_lshlrev_b32 v131, 4, v1\n"
    "s_mul_i32 s8, s11, 163840\n"
    "s_add_u32 s8, s8, 5767168\n"
    "v_lshl_add_u32 v128, v0, 4, s8\n"
    "s_and_b32 s6, s3, 1\n"
    "s_lshl_b32 s8, s14, 11\n"
    "s_lshl_b32 s7, s6, 9\n"
    "s_add_u32 s8, s8, s7\n"
    "s_add_u32 s8, s8, 81920\n"
    "v_lshrrev_b32 v150, 1, v153\n"
    "v_lshl_add_u32 v151, v150, 8, s8\n"
    "v_lshl_add_u32 v151, v152, 4, v151\n"
    "v_and_b32 v130, 1, v153\n"
    "v_xor_b32 v150, s6, v150\n"
    "v_mul_u32_u24 v142, 49152, v150\n"
    "s_lshl_b32 s8, s3, 8\n"
    "v_lshl_add_u32 v141, v152, 4, s8\n"
    "v_add_u32 v142, v142, v141\n"
    "v_lshl_add_u32 v143, v130, 3, v142\n"
    "v_lshl_add_u32 v142, v130, 10, v142\n"
    "v_lshl_add_u32 v130, v130, 3, v151\n"
    "v_lshlrev_b32 v150, 8, v150\n"
    "v_lshl_add_u32 v150, v152, 4, v150\n"
    "v_add_u32 v141, 61440, v150\n"
    "s_lshl_b32 s8, s6, 8\n"
    "s_lshl3_add_u32 s8, s14, s8\n"
    "s_add_u32 s8, s8, 61440\n"
    "v_lshl_add_u32 v150, v152, 4, s8\n"
    "v_lshlrev_b32 v151, 3, v1\n"
    "v_add_u32 v151, 61952, v151\n"
    "v_lshrrev_b32 v154, 2, v152\n"
    "v_cmp_eq_u32 vcc, v154, v153\n"
    "s_nop 1\n"
    "v_cndmask_b32 v140, v151, v150, vcc\n"
    "s_lshl_b32 s7, s14, 11\n"
    "s_xor_b32 s8, s6, 0\n"
    "s_lshl_b32 s8, s8, 10\n"
    "s_add_u32 s8, s8, 122880\n"
    "v_add_u32 v150, s8, v131\n"
    "v_add_u32 v132, s7, v150\n"
    "v_subrev_u32 v133, s7, v150\n"
    "s_xor_b32 s8, s6, 1\n"
    "s_lshl_b32 s8, s8, 10\n"
    "s_add_u32 s8, s8, 122880\n"
    "v_add_u32 v150, s8, v131\n"
    "v_add_u32 v134, s7, v150\n"
    "v_subrev_u32 v135, s7, v150\n"
    "v_subrev_u32 v136, 40960, v132\n"
    "v_subrev_u32 v137, 40960, v133\n"
    "v_subrev_u32 v138, 40960, v134\n"
    "v_subrev_u32 v139, 40960, v135\n"
    "s_mul_i32 s8, s3, 24\n"
    "s_bfe_u32 s7, s2, 0x10005\n"
    "s_add_u32 s8, s8, s7\n"
    "s_lshl_b32 s8, s8, 7\n"
    "s_add_u32 s8, s8, 57344\n"
    "v_min_u32 v150, 11, v1\n"
    "v_lshl_add_u32 v129, v150, 8, s8\n"
    "v_lshlrev_b32 v144, 4, v0\n"
    "v_min_u32 v145, 39, v1\n"
    "s_lshl_b32 s9, s3, 9\n"
    "s_add_u32 s9, s9, 36864\n"
    "v_and_b32 v150, 3, v145\n"
    "v_lshrrev_b32 v145, 2, v145\n"
    "v_lshl_add_u32 v150, v150, 7, s9\n"
    "v_lshl_add_u32 v145, v145, 11, v150\n"
    "v_accvgpr_write_b32 a216, v128\n"
    "v_accvgpr_write_b32 a217, v129\n"
    "v_accvgpr_write_b32 a218, v130\n"
    "v_accvgpr_write_b32 a219, v131\n"
    "v_accvgpr_write_b32 a220, v132\n"
    "v_accvgpr_write_b32 a221, v133\n"
    "v_accvgpr_write_b32 a222, v134\n"
    "v_accvgpr_write_b32 a223, v135\n"
    "v_accvgpr_write_b32 a224, v136\n"
    "v_accvgpr_write_b32 a225, v137\n"
    "v_accvgpr_write_b32 a226, v138\n"
    "v_accvgpr_write_b32 a227, v139\n"
    "v_accvgpr_write_b32 a228, v140\n"
    "v_accvgpr_write_b32 a229, v141\n"
    "v_accvgpr_write_b32 a230, v142\n"
    "v_accvgpr_write_b32 a231, v143\n"
    "v_accvgpr_write_b32 a232, v144\n"
    "v_accvgpr_write_b32 a233, v145\n"
    "s_lshr_b32 s8, s2, 5\n"
    "s_lshl_b32 s8, s8, 2\n"
    "s_bitcmp1_b32 s2, 2\n"
    "s_mov_b32 s6, 0x00013210\n"
    "s_cselect_b32 s6, 0x22231032, s6\n"
    "s_mov_b32 s7, 0x42042531\n"
    "s_cselect_b32 s7, 0x53151420, s7\n"
    "s_lshr_b32 s6, s6, s8\n"
    "s_lshr_b32 s7, s7, s8\n"
    "s_lshl_b32 s6, s6, 12\n"
    "s_and_b32 s7, s7, 15\n"
    "s_and_b32 s16, s6, 0x3000\n"
    "s_add_u32 s6, s6, 4096\n"
    "s_and_b32 s17, s6, 0x3000\n"
    "s_add_u32 s6, s6, 4096\n"
    "s_and_b32 s18, s6, 0x3000\n"
    "s_add_u32 s6, s6, 4096\n"
    "s_and_b32 s19, s6, 0x3000\n"
    "s_lshl_b32 s7, s7, 12\n"
    "s_add_u32 s20, s7, 16384\n"
    "s_add_u32 s21, s7, 20480\n"
    "s_sub_u32 s8, s21, 24576\n"
    "s_cmp_lt_u32 s7, 20480\n"
    "s_cselect_b32 s21, s21, s8\n"
    "s_add_u32 s22, s7, 24576\n"
    "s_sub_u32 s8, s22, 24576\n"
    "s_cmp_lt_u32 s7, 16384\n"
    "s_cselect_b32 s22, s22, s8\n"
    "s_add_u32 s23, s7, 28672\n"
    "s_sub_u32 s8, s23, 24576\n"
    "s_cmp_lt_u32 s7, 12288\n"
    "s_cselect_b32 s23, s23, s8\n"
    "s_add_u32 s24, s7, 32768\n"
    "s_sub_u32 s8, s24, 24576\n"
    "s_cmp_lt_u32 s7, 8192\n"
    "s_cselect_b32 s24, s24, s8\n"
    "s_add_u32 s25, s7, 36864\n"
    "s_sub_u32 s8, s25, 24576\n"
    "s_cmp_lt_u32 s7, 4096\n"
    "s_cselect_b32 s25, s25, s8\n"
    "s_lshl2_add_u32 s8, s12, s3\n"
    "s_mul_i32 s45, s8, 81920\n"
    "s_add_u32 s45, s45, 16646144\n"
    "s_mov_b32 s27, 131072\n"
    "s_mov_b32 s29, 262144\n"
    "s_lshl_b32 s28, s2, 8\n"
    "s_add_u32 s28, s28, 5767168\n"
    "s_mul_i32 s40, s3, 5120\n"
    "s_bfe_u32 s43, s2, 0x30003\n"
    "s_bfe_u32 s46, s2, 0x10006\n"
    "s_lshr_b32 s8, s2, 7\n"
    "s_and_b32 s7, s2, 7\n"
    "s_lshl1_add_u32 s47, s7, s8\n"
    "s_lshl2_add_u32 s8, s47, s3\n"
    "s_lshl_b32 s41, s8, 9\n"
    "s_add_u32 s41, s41, 69632\n"
    "s_mul_i32 s8, s43, 160\n"
    "s_lshl1_add_u32 s42, s47, s46\n"
    "s_mul_i32 s42, s42, 20480\n"
    "s_add_u32 s42, s42, s8\n"
    "s_mul_i32 s30, s47, 393216\n"
    "s_mul_i32 s8, s46, 49152\n"
    "s_add_u32 s30, s30, s8\n"
    "s_mul_i32 s8, s3, 12288\n"
    "s_add_u32 s30, s30, s8\n"
    "s_add_u32 s30, s30, 8388608\n"
    "s_mul_i32 s31, s47, 163840\n"
    "s_mul_i32 s8, s43, 5120\n"
    "s_add_u32 s31, s31, s8\n"
    "s_lshl3_add_u32 s31, s46, s31\n"
    "s_add_u32 s31, s31, 5767168\n"
    "s_waitcnt vmcnt(12)\n"
    "ds_write_b64 v18, v[60:61] offset:4608\n"
    "ds_read_b128 v[80:83], v23 offset:4608\n"
    "ds_read_b128 v[84:87], v23 offset:4624\n"
    "ds_read_b128 v[88:91], v23 offset:4640\n"
    "ds_read_b128 v[92:95], v23 offset:4656\n"
    "s_mov_b32 s6, 0x01000504\n"
    "s_mov_b32 s7, 0x03020706\n"
    "s_waitcnt vmcnt(5)\n"
    "ds_read_b128 v[32:35], v250 offset:6272\n"
    "ds_read_b128 v[36:39], v250 offset:7424\n"
    "ds_read_b128 v[40:43], v250 offset:8576\n"
    "ds_read_b128 v[44:47], v250 offset:5248\n"
    "ds_read_b128 v[48:51], v250 offset:6400\n"
    "ds_read_b128 v[52:55], v250 offset:7552\n"
    "ds_read_b128 v[56:59], v250 offset:8704\n"
    "buffer_load_ushort v26, off, s[36:39], s13\n"
    "s_add_u32 m0, s61, 10240\n"
    "s_nop 0\n"
    "buffer_load_dwordx4 v2, s[32:35], s67 offen offset:0 nt lds\n"
    "buffer_load_dwordx4 v2, s[32:35], s67 offen offset:1024 nt lds\n"
    "buffer_load_dwordx4 v2, s[32:35], s67 offen offset:2048 nt lds\n"
    "buffer_load_dwordx4 v2, s[32:35], s67 offen offset:3072 nt lds\n"
    "s_waitcnt lgkmcnt(5)\n"
    "v_perm_b32 v128, v36, v32, s6\n"
    "v_perm_b32 v129, v36, v32, s7\n"
    "v_perm_b32 v130, v37, v33, s6\n"
    "v_perm_b32 v131, v37, v33, s7\n"
    "v_perm_b32 v132, v38, v34, s6\n"
    "v_perm_b32 v133, v38, v34, s7\n"
    "v_perm_b32 v134, v39, v35, s6\n"
    "v_perm_b32 v135, v39, v35, s7\n"
    "s_waitcnt lgkmcnt(4)\n"
    "v_perm_b32 v136, v40, v36, s6\n"
    "v_perm_b32 v137, v40, v36, s7\n"
    "v_perm_b32 v138, v41, v37, s6\n"
    "v_perm_b32 v139, v41, v37, s7\n"
    "v_perm_b32 v140, v42, v38, s6\n"
    "v_perm_b32 v141, v42, v38, s7\n"
    "v_perm_b32 v142, v43, v39, s6\n"
    "v_perm_b32 v143, v43, v39, s7\n"
    "s_waitcnt lgkmcnt(3)\n"
    "v_perm_b32 v144, v44, v40, s6\n"
    "v_perm_b32 v145, v44, v40, s7\n"
    "v_perm_b32 v146, v45, v41, s6\n"
    "v_perm_b32 v147, v45, v41, s7\n"
    "v_perm_b32 v148, v46, v42, s6\n"
    "v_perm_b32 v149, v46, v42, s7\n"
    "v_perm_b32 v150, v47, v43, s6\n"
    "v_perm_b32 v151, v47, v43, s7\n"
    "s_waitcnt lgkmcnt(2)\n"
    "v_perm_b32 v152, v48, v44, s6\n"
    "v_perm_b32 v153, v48, v44, s7\n"
    "v_perm_b32 v154, v49, v45, s6\n"
    "v_perm_b32 v155, v49, v45, s7\n"
    "v_perm_b32 v156, v50, v46, s6\n"
    "v_perm_b32 v157, v50, v46, s7\n"
    "v_perm_b32 v158, v51, v47, s6\n"
    "v_perm_b32 v159, v51, v47, s7\n"
    "s_waitcnt lgkmcnt(1)\n"
    "v_perm_b32 v160, v52, v48, s6\n"
    "v_perm_b32 v161, v52, v48, s7\n"
    "v_perm_b32 v162, v53, v49, s6\n"
    "v_perm_b32 v163, v53, v49, s7\n"
    "v_perm_b32 v164, v54, v50, s6\n"
    "v_perm_b32 v165, v54, v50, s7\n"
    "v_perm_b32 v166, v55, v51, s6\n"
    "v_perm_b32 v167, v55, v51, s7\n"
    "s_waitcnt lgkmcnt(0)\n"
    "v_perm_b32 v168, v56, v52, s6\n"
    "v_perm_b32 v169, v56, v52, s7\n"
    "v_perm_b32 v170, v57, v53, s6\n"
    "v_perm_b32 v171, v57, v53, s7\n"
    "v_perm_b32 v172, v58, v54, s6\n"
    "v_perm_b32 v173, v58, v54, s7\n"
    "v_perm_b32 v174, v59, v55, s6\n"
    "v_perm_b32 v175, v59, v55, s7\n"
    "v_dot2_f32_bf16 v176, v128, v81, 0\n"
    "v_dot2_f32_bf16 v177, v129, v83, 0\n"
    "v_dot2_f32_bf16 v178, v130, v85, 0\n"
    "v_dot2_f32_bf16 v179, v131, v87, 0\n"
    "v_dot2_f32_bf16 v180, v132, v89, 0\n"
    "v_dot2_f32_bf16 v181, v133, v91, 0\n"
    "v_dot2_f32_bf16 v182, v134, v93, 0\n"
    "v_dot2_f32_bf16 v183, v135, v95, 0\n"
    "v_dot2_f32_bf16 v184, v136, v81, 0\n"
    "v_dot2_f32_bf16 v185, v137, v83, 0\n"
    "v_dot2_f32_bf16 v186, v138, v85, 0\n"
    "v_dot2_f32_bf16 v187, v139, v87, 0\n"
    "v_dot2_f32_bf16 v188, v140, v89, 0\n"
    "v_dot2_f32_bf16 v189, v141, v91, 0\n"
    "v_dot2_f32_bf16 v190, v142, v93, 0\n"
    "v_dot2_f32_bf16 v191, v143, v95, 0\n"
    "v_dot2_f32_bf16 v192, v144, v81, 0\n"
    "v_dot2_f32_bf16 v193, v145, v83, 0\n"
    "v_dot2_f32_bf16 v194, v146, v85, 0\n"
    "v_dot2_f32_bf16 v195, v147, v87, 0\n"
    "v_dot2_f32_bf16 v196, v148, v89, 0\n"
    "v_dot2_f32_bf16 v197, v149, v91, 0\n"
    "v_dot2_f32_bf16 v198, v150, v93, 0\n"
    "v_dot2_f32_bf16 v199, v151, v95, 0\n"
    "v_dot2_f32_bf16 v200, v152, v81, 0\n"
    "v_dot2_f32_bf16 v201, v153, v83, 0\n"
    "v_dot2_f32_bf16 v202, v154, v85, 0\n"
    "v_dot2_f32_bf16 v203, v155, v87, 0\n"
    "v_dot2_f32_bf16 v204, v156, v89, 0\n"
    "v_dot2_f32_bf16 v205, v157, v91, 0\n"
    "v_dot2_f32_bf16 v206, v158, v93, 0\n"
    "v_dot2_f32_bf16 v207, v159, v95, 0\n"
    "v_dot2_f32_bf16 v176, v144, v80, v176\n"
    "v_dot2_f32_bf16 v177, v145, v82, v177\n"
    "v_dot2_f32_bf16 v178, v146, v84, v178\n"
    "v_dot2_f32_bf16 v179, v147, v86, v179\n"
    "v_dot2_f32_bf16 v180, v148, v88, v180\n"
    "v_dot2_f32_bf16 v181, v149, v90, v181\n"
    "v_dot2_f32_bf16 v182, v150, v92, v182\n"
    "v_dot2_f32_bf16 v183, v151, v94, v183\n"
    "v_dot2_f32_bf16 v184, v152, v80, v184\n"
    "v_dot2_f32_bf16 v185, v153, v82, v185\n"
    "v_dot2_f32_bf16 v186, v154, v84, v186\n"
    "v_dot2_f32_bf16 v187, v155, v86, v187\n"
    "v_dot2_f32_bf16 v188, v156, v88, v188\n"
    "v_dot2_f32_bf16 v189, v157, v90, v189\n"
    "v_dot2_f32_bf16 v190, v158, v92, v190\n"
    "v_dot2_f32_bf16 v191, v159, v94, v191\n"
    "v_dot2_f32_bf16 v192, v160, v80, v192\n"
    "v_dot2_f32_bf16 v193, v161, v82, v193\n"
    "v_dot2_f32_bf16 v194, v162, v84, v194\n"
    "v_dot2_f32_bf16 v195, v163, v86, v195\n"
    "v_dot2_f32_bf16 v196, v164, v88, v196\n"
    "v_dot2_f32_bf16 v197, v165, v90, v197\n"
    "v_dot2_f32_bf16 v198, v166, v92, v198\n"
    "v_dot2_f32_bf16 v199, v167, v94, v199\n"
    "v_dot2_f32_bf16 v200, v168, v80, v200\n"
    "v_dot2_f32_bf16 v201, v169, v82, v201\n"
    "v_dot2_f32_bf16 v202, v170, v84, v202\n"
    "v_dot2_f32_bf16 v203, v171, v86, v203\n"
    "v_dot2_f32_bf16 v204, v172, v88, v204\n"
    "v_dot2_f32_bf16 v205, v173, v90, v205\n"
    "v_dot2_f32_bf16 v206, v174, v92, v206\n"
    "v_dot2_f32_bf16 v207, v175, v94, v207\n"
    "s_waitcnt vmcnt(9)\n"
    "v_lshlrev_b32 v128, 16, v64\n"
    "v_and_b32 v129, 0xffff0000, v64\n"
    "v_lshlrev_b32 v130, 16, v65\n"
    "v_and_b32 v131, 0xffff0000, v65\n"
    "v_lshlrev_b32 v132, 16, v66\n"
    "v_and_b32 v133, 0xffff0000, v66\n"
    "v_lshlrev_b32 v134, 16, v67\n"
    "v_and_b32 v135, 0xffff0000, v67\n"
    "v_pk_mul_f32 v[176:177], v[176:177], v[128:129]\n"
    "v_pk_mul_f32 v[178:179], v[178:179], v[130:131]\n"
    "v_pk_mul_f32 v[180:181], v[180:181], v[132:133]\n"
    "v_pk_mul_f32 v[182:183], v[182:183], v[134:135]\n"
    "s_waitcnt vmcnt(8)\n"
    "v_lshlrev_b32 v136, 16, v68\n"
    "v_and_b32 v137, 0xffff0000, v68\n"
    "v_lshlrev_b32 v138, 16, v69\n"
    "v_and_b32 v139, 0xffff0000, v69\n"
    "v_lshlrev_b32 v140, 16, v70\n"
    "v_and_b32 v141, 0xffff0000, v70\n"
    "v_lshlrev_b32 v142, 16, v71\n"
    "v_and_b32 v143, 0xffff0000, v71\n"
    "v_pk_mul_f32 v[184:185], v[184:185], v[136:137]\n"
    "v_pk_mul_f32 v[186:187], v[186:187], v[138:139]\n"
    "v_pk_mul_f32 v[188:189], v[188:189], v[140:141]\n"
    "v_pk_mul_f32 v[190:191], v[190:191], v[142:143]\n"
    "s_waitcnt vmcnt(7)\n"
    "v_lshlrev_b32 v144, 16, v72\n"
    "v_and_b32 v145, 0xffff0000, v72\n"
    "v_lshlrev_b32 v146, 16, v73\n"
    "v_and_b32 v147, 0xffff0000, v73\n"
    "v_lshlrev_b32 v148, 16, v74\n"
    "v_and_b32 v149, 0xffff0000, v74\n"
    "v_lshlrev_b32 v150, 16, v75\n"
    "v_and_b32 v151, 0xffff0000, v75\n"
    "v_pk_mul_f32 v[192:193], v[192:193], v[144:145]\n"
    "v_pk_mul_f32 v[194:195], v[194:195], v[146:147]\n"
    "v_pk_mul_f32 v[196:197], v[196:197], v[148:149]\n"
    "v_pk_mul_f32 v[198:199], v[198:199], v[150:151]\n"
    "s_waitcnt vmcnt(6)\n"
    "v_lshlrev_b32 v152, 16, v76\n"
    "v_and_b32 v153, 0xffff0000, v76\n"
    "v_lshlrev_b32 v154, 16, v77\n"
    "v_and_b32 v155, 0xffff0000, v77\n"
    "v_lshlrev_b32 v156, 16, v78\n"
    "v_and_b32 v157, 0xffff0000, v78\n"
    "v_lshlrev_b32 v158, 16, v79\n"
    "v_and_b32 v159, 0xffff0000, v79\n"
    "v_pk_mul_f32 v[200:201], v[200:201], v[152:153]\n"
    "v_pk_mul_f32 v[202:203], v[202:203], v[154:155]\n"
    "v_pk_mul_f32 v[204:205], v[204:205], v[156:157]\n"
    "v_pk_mul_f32 v[206:207], v[206:207], v[158:159]\n"
    "v_cvt_pk_bf16_f32 v208, v200, v192\n"
    "v_cvt_pk_bf16_f32 v209, v184, v176\n"
    "v_cvt_pk_bf16_f32 v210, v201, v193\n"
    "v_cvt_pk_bf16_f32 v211, v185, v177\n"
    "ds_write2_b64 v19, v[208:209], v[210:211] offset0:0 offset1:2\n"
    "v_cvt_pk_bf16_f32 v212, v202, v194\n"
    "v_cvt_pk_bf16_f32 v213, v186, v178\n"
    "v_cvt_pk_bf16_f32 v214, v203, v195\n"
    "v_cvt_pk_bf16_f32 v215, v187, v179\n"
    "ds_write2_b64 v19, v[212:213], v[214:215] offset0:4 offset1:6\n"
    "v_cvt_pk_bf16_f32 v216, v204, v196\n"
    "v_cvt_pk_bf16_f32 v217, v188, v180\n"
    "v_cvt_pk_bf16_f32 v218, v205, v197\n"
    "v_cvt_pk_bf16_f32 v219, v189, v181\n"
    "ds_write2_b64 v19, v[216:217], v[218:219] offset0:32 offset1:34\n"
    "v_cvt_pk_bf16_f32 v220, v206, v198\n"
    "v_cvt_pk_bf16_f32 v221, v190, v182\n"
    "v_cvt_pk_bf16_f32 v222, v207, v199\n"
    "v_cvt_pk_bf16_f32 v223, v191, v183\n"
    "ds_write2_b64 v19, v[220:221], v[222:223] offset0:36 offset1:38\n"
    "ds_read_b128 v[224:227], v20 offset:0\n"
    "ds_read_b128 v[228:231], v20 offset:1056\n"
    "ds_read_b128 v[232:235], v20 offset:2112\n"
    "ds_read_b128 v[236:239], v20 offset:3168\n"
    "s_mov_b32 s8, 393216\n"
    "s_waitcnt lgkmcnt(3)\n"
    "buffer_store_dwordx4 v[224:227], v21, s[52:55], s8 offen sc1\n"
    "s_mov_b32 s8, 524800\n"
    "s_waitcnt lgkmcnt(2)\n"
    "buffer_store_dwordx4 v[228:231], v21, s[52:55], s8 offen sc1\n"
    "s_mov_b32 s8, 656384\n"
    "s_waitcnt lgkmcnt(1)\n"
    "buffer_store_dwordx4 v[232:235], v21, s[52:55], s8 offen sc1\n"
    "s_mov_b32 s8, 787968\n"
    "s_waitcnt lgkmcnt(0)\n"
    "buffer_store_dwordx4 v[236:239], v21, s[52:55], s8 offen sc1\n"
    "s_mov_b32 s32, 0x22222222\n"
    "s_mov_b32 s33, 0x22222222\n"
    "s_mov_b32 s34, 0x44444444\n"
    "s_mov_b32 s35, 0x44444444\n"
    "s_mov_b32 s36, 0x88888888\n"
    "s_mov_b32 s37, 0x88888888\n"
    "s_mul_i32 s38, s11, 393216\n"
    "s_mul_i32 s8, s12, 3072\n"
    "s_add_u32 s38, s38, s8\n"
    "s_add_u32 s38, s38, 8388608\n"
    "s_bfe_u32 s8, s2, 0x30003\n"
    "s_lshl2_add_u32 s8, s8, s3\n"
    "s_mul_i32 s39, s8, 61440\n"
    "s_add_u32 s39, s39, 14680064\n"
    "s_waitcnt vmcnt(9)\n"
    "v_readfirstlane_b32 s64, v24\n"
    "v_readfirstlane_b32 s65, v25\n"
    "s_add_u32 s64, s64, 0x7f4a7c15\n"
    "s_addc_u32 s65, s65, 0x9e3779b9\n"
    "s_waitcnt vmcnt(0)\n"
    "s_barrier\n"
    "v_mov_b32 v28, s64\n"
    "v_mov_b32 v29, s65\n"
    "v_cvt_f32_u32 v30, v0\n"
    "s_add_u32 s98, s4, 4294934528\n"
    "buffer_store_dwordx2 v[28:29], v30, s[52:55], s98 offen offset:0 sc1\n"
    "ds_read2_b64 v[104:107], v251 offset0:0 offset1:1\n"
    "ds_read2_b64 v[108:111], v251 offset0:64 offset1:65\n"
    "ds_read2_b64 v[112:115], v251 offset0:128 offset1:129\n"
    "ds_read2_b64 v[116:119], v251 offset0:192 offset1:193\n"
    "v_add_u32 v251, 2048, v251\n"
    "ds_read2_b64 v[120:123], v251 offset0:0 offset1:1\n"
    "ds_read2_b64 v[124:127], v251 offset0:64 offset1:65\n"
    "ds_read2_b64 v[240:243], v251 offset0:128 offset1:129\n"
    "ds_read2_b64 v[244:247], v251 offset0:192 offset1:193\n"
    "s_waitcnt lgkmcnt(7)\n"
    "v_lshlrev_b32 v16, 16, v104\n"
    "v_lshlrev_b32 v17, 16, v26\n"
    "v_add_f32 v16, v16, v17\n"
    "v_cvt_pk_bf16_f32 v16, v16, v16\n"
    "s_mov_b32 s8, 0xffff\n"
    "v_bfi_b32 v16, s8, v16, v104\n"
    "v_cmp_eq_u32 vcc, 0, v1\n"
    "s_nop 1\n"
    "v_cndmask_b32 v104, v104, v16, vcc\n"
    "s_mul_i32 s8, s3, 16512\n"
    "v_lshl_add_u32 v20, v1, 4, s8\n"
    "v_lshl_add_u32 v23, v1, 5, s8\n"
    "v_readfirstlane_b32 s6, v104\n"
    "v_readfirstlane_b32 s7, v105\n"
    "v_pk_mov_b32 v[64:65], v[104:105], v[106:107] op_sel:[1,0]\n"
    "v_alignbit_b32 v66, v106, v105, 16\n"
    "v_alignbit_b32 v67, v107, v106, 16\n"
    "v_alignbit_b32 v107, v106, v105, 16\n"
    "v_alignbit_b32 v106, v105, v104, 16\n"
    "ds_write_b128 v23, v[64:67] offset:144\n"
    "ds_write_b128 v23, v[104:107] offset:128\n"
    "s_waitcnt lgkmcnt(8)\n"
    "v_pk_mov_b32 v[68:69], v[108:109], v[110:111] op_sel:[1,0]\n"
    "v_alignbit_b32 v70, v110, v109, 16\n"
    "v_alignbit_b32 v71, v111, v110, 16\n"
    "v_alignbit_b32 v111, v110, v109, 16\n"
    "v_alignbit_b32 v110, v109, v108, 16\n"
    "ds_write_b128 v23, v[68:71] offset:2192\n"
    "ds_write_b128 v23, v[108:111] offset:2176\n"
    "s_waitcnt lgkmcnt(9)\n"
    "v_pk_mov_b32 v[72:73], v[112:113], v[114:115] op_sel:[1,0]\n"
    "v_alignbit_b32 v74, v114, v113, 16\n"
    "v_alignbit_b32 v75, v115, v114, 16\n"
    "v_alignbit_b32 v115, v114, v113, 16\n"
    "v_alignbit_b32 v114, v113, v112, 16\n"
    "ds_write_b128 v23, v[72:75] offset:4240\n"
    "ds_write_b128 v23, v[112:115] offset:4224\n"
    "s_waitcnt lgkmcnt(10)\n"
    "v_pk_mov_b32 v[76:77], v[116:117], v[118:119] op_sel:[1,0]\n"
    "v_alignbit_b32 v78, v118, v117, 16\n"
    "v_alignbit_b32 v79, v119, v118, 16\n"
    "v_alignbit_b32 v119, v118, v117, 16\n"
    "v_alignbit_b32 v118, v117, v116, 16\n"
    "ds_write_b128 v23, v[76:79] offset:6288\n"
    "ds_write_b128 v23, v[116:119] offset:6272\n"
    "s_waitcnt lgkmcnt(11)\n"
    "v_pk_mov_b32 v[80:81], v[120:121], v[122:123] op_sel:[1,0]\n"
    "v_alignbit_b32 v82, v122, v121, 16\n"
    "v_alignbit_b32 v83, v123, v122, 16\n"
    "v_alignbit_b32 v123, v122, v121, 16\n"
    "v_alignbit_b32 v122, v121, v120, 16\n"
    "ds_write_b128 v23, v[80:83] offset:8336\n"
    "ds_write_b128 v23, v[120:123] offset:8320\n"
    "s_waitcnt lgkmcnt(12)\n"
    "v_pk_mov_b32 v[84:85], v[124:125], v[126:127] op_sel:[1,0]\n"
    "v_alignbit_b32 v86, v126, v125, 16\n"
    "v_alignbit_b32 v87, v127, v126, 16\n"
    "v_alignbit_b32 v127, v126, v125, 16\n"
    "v_alignbit_b32 v126, v125, v124, 16\n"
    "ds_write_b128 v23, v[84:87] offset:10384\n"
    "ds_write_b128 v23, v[124:127] offset:10368\n"
    "s_waitcnt lgkmcnt(13)\n"
    "v_pk_mov_b32 v[88:89], v[240:241], v[242:243] op_sel:[1,0]\n"
    "v_alignbit_b32 v90, v242, v241, 16\n"
    "v_alignbit_b32 v91, v243, v242, 16\n"
    "v_alignbit_b32 v243, v242, v241, 16\n"
    "v_alignbit_b32 v242, v241, v240, 16\n"
    "ds_write_b128 v23, v[88:91] offset:12432\n"
    "ds_write_b128 v23, v[240:243] offset:12416\n"
    "s_waitcnt lgkmcnt(14)\n"
    "v_pk_mov_b32 v[92:93], v[244:245], v[246:247] op_sel:[1,0]\n"
    "v_alignbit_b32 v94, v246, v245, 16\n"
    "v_alignbit_b32 v95, v247, v246, 16\n"
    "v_alignbit_b32 v247, v246, v245, 16\n"
    "v_alignbit_b32 v246, v245, v244, 16\n"
    "ds_write_b128 v23, v[92:95] offset:14480\n"
    "ds_write_b128 v23, v[244:247] offset:14464\n"
    "v_mov_b32 v16, 0\n"
    "v_mov_b32 v21, s6\n"
    "v_mov_b32 v22, s7\n"
    "v_cmp_eq_u32 vcc, 7, v1\n"
    "s_nop 1\n"
    "v_cndmask_b32 v17, v16, v21, vcc\n"
    "v_cndmask_b32 v22, v16, v22, vcc\n"
    "v_cmp_eq_u32 vcc, 6, v1\n"
    "s_nop 1\n"
    "v_cndmask_b32 v22, v22, v21, vcc\n"
    "v_lshlrev_b32 v18, 16, v17\n"
    "v_alignbit_b32 v19, v22, v17, 16\n"
    "s_mov_b64 exec, 0xff\n"
    "ds_write_b128 v20, v[16:19] offset:0\n"
    "s_mov_b64 exec, -1\n"
    "ds_read_b64 v[128:129], v11 offset:0\n"
    "ds_read_b64 v[130:131], v11 offset:32\n"
    "ds_read_b64 v[132:133], v11 offset:256\n"
    "ds_read_b64 v[134:135], v11 offset:288\n"
    "ds_read_b64 v[136:137], v11 offset:512\n"
    "ds_read_b64 v[138:139], v11 offset:544\n"
    "ds_read_b64 v[140:141], v11 offset:768\n"
    "ds_read_b64 v[142:143], v11 offset:800\n"
    "ds_read_b64 v[144:145], v11 offset:1024\n"
    "ds_read_b64 v[146:147], v11 offset:1056\n"
    "ds_read_b64 v[148:149], v11 offset:1280\n"
    "ds_read_b64 v[150:151], v11 offset:1312\n"
    "ds_read_b64 v[152:153], v11 offset:1536\n"
    "ds_read_b64 v[154:155], v11 offset:1568\n"
    "ds_read_b64 v[156:157], v11 offset:1792\n"
    "ds_read_b64 v[158:159], v11 offset:1824\n"
    "ds_read_b64 v[160:161], v11 offset:2048\n"
    "ds_read_b64 v[162:163], v11 offset:2080\n"
    "ds_read_b64 v[164:165], v11 offset:2304\n"
    "ds_read_b64 v[166:167], v11 offset:2336\n"
    "ds_read_b64 v[168:169], v11 offset:2560\n"
    "ds_read_b64 v[170:171], v11 offset:2592\n"
    "ds_read_b64 v[172:173], v11 offset:2816\n"
    "ds_read_b64 v[174:175], v11 offset:2848\n"
    "ds_read_b64 v[176:177], v11 offset:3072\n"
    "ds_read_b64 v[178:179], v11 offset:3104\n"
    "ds_read_b64 v[180:181], v11 offset:3328\n"
    "ds_read_b64 v[182:183], v11 offset:3360\n"
    "ds_read_b64 v[184:185], v11 offset:3584\n"
    "ds_read_b64 v[186:187], v11 offset:3616\n"
    "ds_read_b64 v[188:189], v11 offset:3840\n"
    "ds_read_b64 v[190:191], v11 offset:3872\n"
    "buffer_load_dwordx2 v[100:101], v98, s[52:55], 0 offen sc1\n"
    "ds_read_b64 v[192:193], v11 offset:4096\n"
    "ds_read_b64 v[194:195], v11 offset:4128\n"
    "ds_read_b64 v[196:197], v11 offset:4352\n"
    "ds_read_b64 v[198:199], v11 offset:4384\n"
    "ds_read_b64 v[200:201], v11 offset:4608\n"
    "ds_read_b64 v[202:203], v11 offset:4640\n"
    "ds_read_b64 v[204:205], v11 offset:4864\n"
    "ds_read_b64 v[206:207], v11 offset:4896\n"
    "ds_read_b64 v[208:209], v11 offset:5120\n"
    "ds_read_b64 v[210:211], v11 offset:5152\n"
    "ds_read_b64 v[212:213], v11 offset:5376\n"
    "ds_read_b64 v[214:215], v11 offset:5408\n"
    "ds_read_b64 v[216:217], v11 offset:5632\n"
    "ds_read_b64 v[218:219], v11 offset:5664\n"
    "ds_read_b64 v[220:221], v11 offset:5888\n"
    "ds_read_b64 v[222:223], v11 offset:5920\n"
    "ds_read_b64 v[224:225], v11 offset:6144\n"
    "ds_read_b64 v[226:227], v11 offset:6176\n"
    "ds_read_b64 v[228:229], v11 offset:6400\n"
    "ds_read_b64 v[230:231], v11 offset:6432\n"
    "ds_read_b64 v[232:233], v11 offset:6656\n"
    "ds_read_b64 v[234:235], v11 offset:6688\n"
    "ds_read_b64 v[236:237], v11 offset:6912\n"
    "ds_read_b64 v[238:239], v11 offset:6944\n"
    "ds_read_b64 v[240:241], v11 offset:7168\n"
    "ds_read_b64 v[242:243], v11 offset:7200\n"
    "ds_read_b64 v[244:245], v11 offset:7424\n"
    "ds_read_b64 v[246:247], v11 offset:7456\n"
    "ds_read_b64 v[248:249], v11 offset:7680\n"
    "ds_read_b64 v[250:251], v11 offset:7712\n"
    "ds_read_b64 v[252:253], v11 offset:7936\n"
    "ds_read_b64 v[254:255], v11 offset:7968\n"
    "v_lshlrev_b32 v92, 4, v0\n"
    "s_mul_i32 s66, s2, 32896\n"
    "s_add_u32 s66, s66, 393216\n"
    "s_waitcnt vmcnt(0)\n"
    "v_cmp_ne_u64 vcc, s[64:65], v[100:101]\n"
    "s_cmp_eq_u32 vcc_lo, 0\n"
    "s_cbranch_scc1 .L1_%=\n"
    "s_mov_b32 s98, 100000\n"
    ".L3_%=:\n"
    "s_mov_b32 exec_hi, 0\n"
    "buffer_load_dwordx2 v[100:101], v98, s[52:55], 0 offen sc1\n"
    "s_mov_b32 exec_hi, -1\n"
    "s_waitcnt vmcnt(0)\n"
    "v_cmp_ne_u64 vcc, s[64:65], v[100:101]\n"
    "s_cmp_eq_u32 vcc_lo, 0\n"
    "s_cbranch_scc1 .L1_%=\n"
    "s_sub_u32 s98, s98, 1\n"
    "s_cbranch_scc0 .L3_%=\n"
    ".L1_%=:\n"
    "buffer_load_dwordx2 v[100:101], v98, s[52:55], 0 offen sc1\n"
    "buffer_load_dwordx4 v[64:67], v92, s[52:55], s66 offen sc1\n"
    "s_add_u32 s8, s66, 4096\n"
    "buffer_load_dwordx4 v[68:71], v92, s[52:55], s8 offen sc1\n"
    "s_add_u32 s8, s66, 8192\n"
    "buffer_load_dwordx4 v[72:75], v92, s[52:55], s8 offen sc1\n"
    "s_add_u32 s8, s66, 12288\n"
    "buffer_load_dwordx4 v[76:79], v92, s[52:55], s8 offen sc1\n"
    "s_waitcnt vmcnt(4)\n"
    "v_cmp_ne_u64 vcc, s[64:65], v[100:101]\n"
    "s_cmp_eq_u32 vcc_hi, 0\n"
    "s_cbranch_scc1 .L4_%=\n"
    "s_mov_b32 s98, 100000\n"
    ".L6_%=:\n"
    "s_mov_b32 exec_lo, 0\n"
    "buffer_load_dwordx2 v[100:101], v98, s[52:55], 0 offen sc1\n"
    "s_mov_b32 exec_lo, -1\n"
    "s_waitcnt vmcnt(0)\n"
    "v_cmp_ne_u64 vcc, s[64:65], v[100:101]\n"
    "s_cmp_eq_u32 vcc_hi, 0\n"
    "s_cbranch_scc1 .L4_%=\n"
    "s_sub_u32 s98, s98, 1\n"
    "s_cbranch_scc0 .L6_%=\n"
    ".L4_%=:\n"
    "s_add_u32 s67, s66, 16384\n"
    "buffer_load_dwordx4 v[32:35], v92, s[52:55], s67 offen sc1\n"
    "s_mov_b32 s67, 102400\n"
    "buffer_load_dwordx4 v[16:19], v92, s[52:55], s67 offen sc1\n"
    "s_lshl1_add_u32 s44, s16, s45\n"
    "buffer_load_dwordx4 a[0:3], v2, s[52:55], s44 offen offset:0 sc1\n"
    "buffer_load_dwordx4 a[4:7], v2, s[52:55], s44 offen offset:1024 sc1\n"
    "buffer_load_dwordx4 a[8:11], v2, s[52:55], s44 offen offset:2048 sc1\n"
    "buffer_load_dwordx4 a[12:15], v2, s[52:55], s44 offen offset:3072 sc1\n"
    "s_add_u32 s44, s44, 4096\n"
    "buffer_load_dwordx4 a[16:19], v2, s[52:55], s44 offen offset:0 sc1\n"
    "buffer_load_dwordx4 a[20:23], v2, s[52:55], s44 offen offset:1024 sc1\n"
    "s_waitcnt vmcnt(11)\n"
    "ds_write_b128 v97, v[64:67] offset:0\n"
    "s_waitcnt vmcnt(10)\n"
    "ds_write_b128 v97, v[68:71] offset:1024\n"
    "s_waitcnt vmcnt(9)\n"
    "ds_write_b128 v97, v[72:75] offset:2048\n"
    "s_waitcnt vmcnt(8)\n"
    "ds_write_b128 v97, v[76:79] offset:3072\n"
    "s_waitcnt lgkmcnt(0)\n"
    "s_barrier\n"
    "ds_read_b128 v[96:99], v12 offset:448\n"
    "ds_read_b128 v[100:103], v12 offset:960\n"
    "ds_read_b128 v[104:107], v12 offset:384\n"
    "ds_read_b128 v[108:111], v12 offset:896\n"
    "ds_read_b128 v[112:115], v12 offset:320\n"
    "ds_read_b128 v[116:119], v12 offset:832\n"
    "ds_read_b128 v[120:123], v12 offset:256\n"
    "ds_read_b128 v[124:127], v12 offset:768\n"
    "s_waitcnt lgkmcnt(7)\n"
    "v_mfma_f32_16x16x32_bf16 v[64:67], v[128:131], v[96:99], 0\n"
    "s_add_u32 s67, s66, 20480\n"
    "v_mfma_f32_16x16x32_bf16 v[68:71], v[160:163], v[96:99], 0\n"
    "s_waitcnt lgkmcnt(6)\n"
    "ds_read_b128 v[96:99], v12 offset:192\n"
    "v_mfma_f32_16x16x32_bf16 v[68:71], v[128:131], v[100:103], v[68:71]\n"
    "s_waitcnt lgkmcnt(6)\n"
    "ds_read_b128 v[100:103], v12 offset:704\n"
    "v_mfma_f32_16x16x32_bf16 v[64:67], v[132:135], v[104:107], v[64:67]\n"
    "buffer_load_dwordx4 v[36:39], v92, s[52:55], s67 offen sc1\n"
    "v_mfma_f32_16x16x32_bf16 v[68:71], v[164:167], v[104:107], v[68:71]\n"
    "s_waitcnt lgkmcnt(6)\n"
    "ds_read_b128 v[104:107], v12 offset:128\n"
    "s_mov_b32 s67, 106496\n"
    "v_mfma_f32_16x16x32_bf16 v[68:71], v[132:135], v[108:111], v[68:71]\n"
    "s_waitcnt lgkmcnt(6)\n"
    "ds_read_b128 v[108:111], v12 offset:640\n"
    "v_mfma_f32_16x16x32_bf16 v[64:67], v[136:139], v[112:115], v[64:67]\n"
    "buffer_load_dwordx4 v[20:23], v92, s[52:55], s67 offen sc1\n"
    "v_mfma_f32_16x16x32_bf16 v[68:71], v[168:171], v[112:115], v[68:71]\n"
    "s_waitcnt lgkmcnt(6)\n"
    "s_add_u32 s67, s66, 24576\n"
    "v_mfma_f32_16x16x32_bf16 v[68:71], v[136:139], v[116:119], v[68:71]\n"
    "s_waitcnt lgkmcnt(5)\n"
    "ds_read_b128 v[112:115], v12 offset:64\n"
    "ds_read_b128 v[116:119], v12 offset:576\n"
    "v_mfma_f32_16x16x32_bf16 v[64:67], v[140:143], v[120:123], v[64:67]\n"
    "buffer_load_dwordx4 v[40:43], v92, s[52:55], s67 offen sc1\n"
    "v_mfma_f32_16x16x32_bf16 v[68:71], v[172:175], v[120:123], v[68:71]\n"
    "s_waitcnt lgkmcnt(6)\n"
    "ds_read_b128 v[120:123], v12 offset:0\n"
    "s_mov_b32 s67, 110592\n"
    "v_mfma_f32_16x16x32_bf16 v[68:71], v[140:143], v[124:127], v[68:71]\n"
    "s_waitcnt lgkmcnt(6)\n"
    "s_waitcnt vmcnt(10)\n"
    "v_lshlrev_b32 v48, 16, v32\n"
    "v_and_b32 v49, 0xffff0000, v32\n"
    "v_lshlrev_b32 v50, 16, v33\n"
    "v_mfma_f32_16x16x32_bf16 v[64:67], v[144:147], v[96:99], v[64:67]\n"
    "ds_read_b128 v[124:127], v12 offset:512\n"
    "v_and_b32 v51, 0xffff0000, v33\n"
    "v_lshlrev_b32 v52, 16, v34\n"
    "v_mfma_f32_16x16x32_bf16 v[68:71], v[176:179], v[96:99], v[68:71]\n"
    "s_waitcnt lgkmcnt(6)\n"
    "ds_read_b128 v[96:99], v12 offset:448\n"
    "v_and_b32 v53, 0xffff0000, v34\n"
    "v_lshlrev_b32 v54, 16, v35\n"
    "v_mfma_f32_16x16x32_bf16 v[68:71], v[144:147], v[100:103], v[68:71]\n"
    "s_waitcnt lgkmcnt(6)\n"
    "ds_read_b128 v[100:103], v12 offset:960\n"
    "v_and_b32 v55, 0xffff0000, v35\n"
    "v_mfma_f32_16x16x32_bf16 v[64:67], v[148:151], v[104:107], v[64:67]\n"
    "buffer_load_dwordx4 v[24:27], v92, s[52:55], s67 offen sc1\n"
    "v_mfma_f32_16x16x32_bf16 v[68:71], v[180:183], v[104:107], v[68:71]\n"
    "s_waitcnt lgkmcnt(6)\n"
    "ds_read_b128 v[104:107], v12 offset:1472\n"
    "s_add_u32 s67, s66, 28672\n"
    "v_mfma_f32_16x16x32_bf16 v[68:71], v[148:151], v[108:111], v[68:71]\n"
    "s_waitcnt lgkmcnt(6)\n"
    "ds_read_b128 v[108:111], v12 offset:1984\n"
    "v_mfma_f32_16x16x32_bf16 v[64:67], v[152:155], v[112:115], v[64:67]\n"
    "buffer_load_dwordx4 v[44:47], v92, s[52:55], s67 offen sc1\n"
    "v_mfma_f32_16x16x32_bf16 v[68:71], v[184:187], v[112:115], v[68:71]\n"
    "s_waitcnt lgkmcnt(6)\n"
    "s_mov_b32 s67, 114688\n"
    "v_mfma_f32_16x16x32_bf16 v[68:71], v[152:155], v[116:119], v[68:71]\n"
    "s_waitcnt lgkmcnt(5)\n"
    "ds_read_b128 v[112:115], v12 offset:384\n"
    "ds_read_b128 v[116:119], v12 offset:896\n"
    "v_mfma_f32_16x16x32_bf16 v[64:67], v[156:159], v[120:123], v[64:67]\n"
    "buffer_load_dwordx4 v[28:31], v92, s[52:55], s67 offen sc1\n"
    "v_mfma_f32_16x16x32_bf16 v[68:71], v[188:191], v[120:123], v[68:71]\n"
    "s_waitcnt lgkmcnt(6)\n"
    "ds_read_b128 v[120:123], v12 offset:1408\n"
    "s_lshl1_add_u32 s44, s17, s45\n"
    "v_mfma_f32_16x16x32_bf16 v[68:71], v[156:159], v[124:127], v[68:71]\n"
    "s_waitcnt lgkmcnt(6)\n"
    "s_waitcnt vmcnt(12)\n"
    "v_fma_f32 v56, v48, v16, v17\n"
    "v_fma_f32 v57, v49, v16, v17\n"
    "v_fma_f32 v58, v50, v16, v17\n"
    "v_fma_f32 v59, v51, v16, v17\n"
    "v_mfma_f32_16x16x32_bf16 v[72:75], v[192:195], v[96:99], 0\n"
    "ds_read_b128 v[124:127], v12 offset:1920\n"
    "v_fma_f32 v60, v52, v18, v19\n"
    "v_fma_f32 v61, v53, v18, v19\n"
    "v_fma_f32 v62, v54, v18, v19\n"
    "v_mfma_f32_16x16x32_bf16 v[76:79], v[224:227], v[96:99], 0\n"
    "s_waitcnt lgkmcnt(6)\n"
    "ds_read_b128 v[96:99], v12 offset:320\n"
    "v_fma_f32 v63, v55, v18, v19\n"
    "v_cvt_pk_bf16_f32 v64, v64, v65\n"
    "v_cvt_pk_bf16_f32 v65, v66, v67\n"
    "v_mfma_f32_16x16x32_bf16 v[72:75], v[160:163], v[100:103], v[72:75]\n"
    "ds_write_b64 v13, v[64:65] offset:16512\n"
    "v_mfma_f32_16x16x32_bf16 v[76:79], v[192:195], v[100:103], v[76:79]\n"
    "s_waitcnt lgkmcnt(7)\n"
    "buffer_load_dwordx4 a[24:27], v2, s[52:55], s44 offen offset:0 sc1\n"
    "v_cvt_pk_bf16_f32 v68, v68, v69\n"
    "v_cvt_pk_bf16_f32 v69, v70, v71\n"
    "v_mfma_f32_16x16x32_bf16 v[72:75], v[128:131], v[104:107], v[72:75]\n"
    "ds_read_b128 v[100:103], v12 offset:832\n"
    "v_mfma_f32_16x16x32_bf16 v[76:79], v[160:163], v[104:107], v[76:79]\n"
    "s_waitcnt lgkmcnt(7)\n"
    "ds_read_b128 v[104:107], v12 offset:1344\n"
    "ds_write_b64 v13, v[68:69] offset:17024\n"
    "s_waitcnt lgkmcnt(0)\n"
    "s_barrier\n"
    "v_mfma_f32_16x16x32_bf16 v[76:79], v[128:131], v[108:111], v[76:79]\n"
    "ds_read_b128 v[108:111], v12 offset:1856\n"
    "v_mfma_f32_16x16x32_bf16 v[72:75], v[196:199], v[112:115], v[72:75]\n"
    "buffer_load_dwordx4 a[28:31], v2, s[52:55], s44 offen offset:1024 sc1\n"
    "v_mfma_f32_16x16x32_bf16 v[76:79], v[228:231], v[112:115], v[76:79]\n"
    "ds_read_b128 v[112:115], v12 offset:256\n"
    "v_mfma_f32_16x16x32_bf16 v[72:75], v[164:167], v[116:119], v[72:75]\n"
    "ds_read_b64_tr_b16 v[64:65], v14 offset:16512\n"
    "v_mfma_f32_16x16x32_bf16 v[76:79], v[196:199], v[116:119], v[76:79]\n"
    "ds_read_b128 v[116:119], v12 offset:768\n"
    "v_mfma_f32_16x16x32_bf16 v[72:75], v[132:135], v[120:123], v[72:75]\n"
    "ds_read_b64_tr_b16 v[66:67], v14 offset:16544\n"
    "v_mfma_f32_16x16x32_bf16 v[76:79], v[164:167], v[120:123], v[76:79]\n"
    "ds_read_b128 v[120:123], v12 offset:1280\n"
    "v_mfma_f32_16x16x32_bf16 v[76:79], v[132:135], v[124:127], v[76:79]\n"
    "ds_read_b128 v[124:127], v12 offset:1792\n"
    "v_mfma_f32_16x16x32_bf16 v[72:75], v[200:203], v[96:99], v[72:75]\n"
    "buffer_load_dwordx4 a[32:35], v2, s[52:55], s44 offen offset:2048 sc1\n"
    "v_mfma_f32_16x16x32_bf16 v[76:79], v[232:235], v[96:99], v[76:79]\n"
    "s_waitcnt lgkmcnt(4)\n"
    "v_lshlrev_b32 v68, 16, v64\n"
    "v_and_b32 v69, 0xffff0000, v64\n"
    "v_fmac_f32 v48, v56, v68\n"
    "v_mfma_f32_16x16x32_bf16 v[72:75], v[168:171], v[100:103], v[72:75]\n"
    "ds_read_b128 v[96:99], v12 offset:192\n"
    "v_fmac_f32 v49, v57, v69\n"
    "v_cvt_pk_bf16_f32 v64, v48, v49\n"
    "v_mfma_f32_16x16x32_bf16 v[76:79], v[200:203], v[100:103], v[76:79]\n"
    "ds_read_b128 v[100:103], v12 offset:704\n"
    "v_lshlrev_b32 v70, 16, v65\n"
    "v_and_b32 v71, 0xffff0000, v65\n"
    "v_mfma_f32_16x16x32_bf16 v[72:75], v[136:139], v[104:107], v[72:75]\n"
    "buffer_load_dwordx4 a[36:39], v2, s[52:55], s44 offen offset:3072 sc1\n"
    "v_fmac_f32 v50, v58, v70\n"
    "v_mfma_f32_16x16x32_bf16 v[76:79], v[168:171], v[104:107], v[76:79]\n"
    "s_add_u32 s44, s44, 4096\n"
    "v_fmac_f32 v51, v59, v71\n"
    "v_cvt_pk_bf16_f32 v65, v50, v51\n"
    "v_mfma_f32_16x16x32_bf16 v[76:79], v[136:139], v[108:111], v[76:79]\n"
    "ds_read_b128 v[104:107], v12 offset:1216\n"
    "ds_read_b128 v[108:111], v12 offset:1728\n"
    "v_mfma_f32_16x16x32_bf16 v[72:75], v[204:207], v[112:115], v[72:75]\n"
    "buffer_load_dwordx4 a[40:43], v2, s[52:55], s44 offen offset:0 sc1\n"
    "v_mfma_f32_16x16x32_bf16 v[76:79], v[236:239], v[112:115], v[76:79]\n"
    "s_waitcnt lgkmcnt(7)\n"
    "ds_read_b128 v[112:115], v12 offset:128\n"
    "v_mfma_f32_16x16x32_bf16 v[72:75], v[172:175], v[116:119], v[72:75]\n"
    "s_waitcnt lgkmcnt(7)\n"
    "v_lshlrev_b32 v68, 16, v66\n"
    "v_and_b32 v69, 0xffff0000, v66\n"
    "v_fmac_f32 v52, v60, v68\n"
    "v_fmac_f32 v53, v61, v69\n"
    "v_mfma_f32_16x16x32_bf16 v[76:79], v[204:207], v[116:119], v[76:79]\n"
    "s_waitcnt lgkmcnt(6)\n"
    "ds_read_b128 v[116:119], v12 offset:640\n"
    "v_cvt_pk_bf16_f32 v66, v52, v53\n"
    "v_lshlrev_b32 v70, 16, v67\n"
    "v_mfma_f32_16x16x32_bf16 v[72:75], v[140:143], v[120:123], v[72:75]\n"
    "buffer_load_dwordx4 a[44:47], v2, s[52:55], s44 offen offset:1024 sc1\n"
    "v_and_b32 v71, 0xffff0000, v67\n"
    "v_fmac_f32 v54, v62, v70\n"
    "v_mfma_f32_16x16x32_bf16 v[76:79], v[172:175], v[120:123], v[76:79]\n"
    "s_waitcnt lgkmcnt(6)\n"
    "ds_read_b128 v[120:123], v12 offset:1152\n"
    "v_fmac_f32 v55, v63, v71\n"
    "v_cvt_pk_bf16_f32 v67, v54, v55\n"
    "v_mfma_f32_16x16x32_bf16 v[76:79], v[140:143], v[124:127], v[76:79]\n"
    "s_waitcnt lgkmcnt(6)\n"
    "s_add_u32 s9, s28, 0\n"
    "buffer_store_dwordx4 v[64:67], v15, s[52:55], s9 offen sc1\n"
    "v_mfma_f32_16x16x32_bf16 v[72:75], v[208:211], v[96:99], v[72:75]\n"
    "ds_read_b128 v[124:127], v12 offset:1664\n"
    "v_mfma_f32_16x16x32_bf16 v[76:79], v[240:243], v[96:99], v[76:79]\n"
    "s_waitcnt lgkmcnt(6)\n"
    "ds_read_b128 v[96:99], v12 offset:64\n"
    "v_mfma_f32_16x16x32_bf16 v[72:75], v[176:179], v[100:103], v[72:75]\n"
    "s_waitcnt vmcnt(12)\n"
    "v_lshlrev_b32 v48, 16, v36\n"
    "v_and_b32 v49, 0xffff0000, v36\n"
    "v_lshlrev_b32 v50, 16, v37\n"
    "v_mfma_f32_16x16x32_bf16 v[76:79], v[208:211], v[100:103], v[76:79]\n"
    "s_waitcnt lgkmcnt(6)\n"
    "ds_read_b128 v[100:103], v12 offset:576\n"
    "v_and_b32 v51, 0xffff0000, v37\n"
    "v_mfma_f32_16x16x32_bf16 v[72:75], v[144:147], v[104:107], v[72:75]\n"
    "v_lshlrev_b32 v52, 16, v38\n"
    "v_and_b32 v53, 0xffff0000, v38\n"
    "v_mfma_f32_16x16x32_bf16 v[76:79], v[176:179], v[104:107], v[76:79]\n"
    "s_waitcnt lgkmcnt(6)\n"
    "ds_read_b128 v[104:107], v12 offset:1088\n"
    "v_lshlrev_b32 v54, 16, v39\n"
    "v_mfma_f32_16x16x32_bf16 v[76:79], v[144:147], v[108:111], v[76:79]\n"
    "s_waitcnt lgkmcnt(6)\n"
    "ds_read_b128 v[108:111], v12 offset:1600\n"
    "v_and_b32 v55, 0xffff0000, v39\n"
    "v_mfma_f32_16x16x32_bf16 v[72:75], v[212:215], v[112:115], v[72:75]\n"
    "s_waitcnt vmcnt(11)\n"
    "v_fma_f32 v56, v48, v20, v21\n"
    "v_fma_f32 v57, v49, v20, v21\n"
    "v_mfma_f32_16x16x32_bf16 v[76:79], v[244:247], v[112:115], v[76:79]\n"
    "s_waitcnt lgkmcnt(6)\n"
    "ds_read_b128 v[112:115], v12 offset:0\n"
    "v_fma_f32 v58, v50, v20, v21\n"
    "v_mfma_f32_16x16x32_bf16 v[72:75], v[180:183], v[116:119], v[72:75]\n"
    "v_fma_f32 v59, v51, v20, v21\n"
    "v_fma_f32 v60, v52, v22, v23\n"
    "v_mfma_f32_16x16x32_bf16 v[76:79], v[212:215], v[116:119], v[76:79]\n"
    "s_waitcnt lgkmcnt(6)\n"
    "ds_read_b128 v[116:119], v12 offset:512\n"
    "v_fma_f32 v61, v53, v22, v23\n"
    "v_mfma_f32_16x16x32_bf16 v[72:75], v[148:151], v[120:123], v[72:75]\n"
    "ds_read_b64 a[240:241], v11 offset:8192\n"
    "v_fma_f32 v62, v54, v22, v23\n"
    "v_mfma_f32_16x16x32_bf16 v[76:79], v[180:183], v[120:123], v[76:79]\n"
    "s_waitcnt lgkmcnt(7)\n"
    "ds_read_b128 v[120:123], v12 offset:1024\n"
    "v_fma_f32 v63, v55, v22, v23\n"
    "v_mfma_f32_16x16x32_bf16 v[76:79], v[148:151], v[124:127], v[76:79]\n"
    "s_waitcnt lgkmcnt(7)\n"
    "ds_read_b128 v[124:127], v12 offset:1536\n"
    "v_mfma_f32_16x16x32_bf16 v[72:75], v[216:219], v[96:99], v[72:75]\n"
    "ds_read_b64 a[242:243], v11 offset:8224\n"
    "v_mfma_f32_16x16x32_bf16 v[76:79], v[248:251], v[96:99], v[76:79]\n"
    "s_waitcnt lgkmcnt(8)\n"
    "ds_read_b128 v[96:99], v12 offset:448\n"
    "v_mfma_f32_16x16x32_bf16 v[72:75], v[184:187], v[100:103], v[72:75]\n"
    "ds_read_b64 a[244:245], v11 offset:10240\n"
    "v_mfma_f32_16x16x32_bf16 v[76:79], v[216:219], v[100:103], v[76:79]\n"
    "s_waitcnt lgkmcnt(9)\n"
    "ds_read_b128 v[100:103], v12 offset:960\n"
    "v_mfma_f32_16x16x32_bf16 v[72:75], v[152:155], v[104:107], v[72:75]\n"
    "ds_read_b64 a[246:247], v11 offset:10272\n"
    "v_mfma_f32_16x16x32_bf16 v[76:79], v[184:187], v[104:107], v[76:79]\n"
    "s_waitcnt lgkmcnt(10)\n"
    "ds_read_b128 v[104:107], v12 offset:1472\n"
    "v_mfma_f32_16x16x32_bf16 v[76:79], v[152:155], v[108:111], v[76:79]\n"
    "s_waitcnt lgkmcnt(10)\n"
    "ds_read_b128 v[108:111], v12 offset:1984\n"
    "v_mfma_f32_16x16x32_bf16 v[72:75], v[220:223], v[112:115], v[72:75]\n"
    "ds_read_b64 a[248:249], v11 offset:12288\n"
    "v_mfma_f32_16x16x32_bf16 v[76:79], v[252:255], v[112:115], v[76:79]\n"
    "s_waitcnt lgkmcnt(11)\n"
    "ds_read_b128 v[112:115], v12 offset:2496\n"
    "v_mfma_f32_16x16x32_bf16 v[72:75], v[188:191], v[116:119], v[72:75]\n"
    "ds_read_b64 a[250:251], v11 offset:12320\n"
    "v_mfma_f32_16x16x32_bf16 v[76:79], v[220:223], v[116:119], v[76:79]\n"
    "s_waitcnt lgkmcnt(11)\n"
    "ds_read_b128 v[116:119], v12 offset:3008\n"
    "v_mfma_f32_16x16x32_bf16 v[72:75], v[156:159], v[120:123], v[72:75]\n"
    "v_mfma_f32_16x16x32_bf16 v[76:79], v[188:191], v[120:123], v[76:79]\n"
    "s_waitcnt lgkmcnt(11)\n"
    "ds_read_b128 v[120:123], v12 offset:384\n"
    "v_mfma_f32_16x16x32_bf16 v[76:79], v[156:159], v[124:127], v[76:79]\n"
    "s_waitcnt lgkmcnt(10)\n"
    "ds_read_b128 v[124:127], v12 offset:896\n"
    "v_mfma_f32_16x16x32_bf16 v[80:83], a[240:243], v[96:99], 0\n"
    "s_waitcnt lgkmcnt(8)\n"
    "ds_read_b64 a[252:253], v11 offset:14336\n"
    "v_mfma_f32_16x16x32_bf16 v[84:87], a[244:247], v[96:99], 0\n"
    "ds_read_b128 v[96:99], v12 offset:1408\n"
    "ds_read_b64 a[244:245], v11 offset:10496\n"
    "v_cvt_pk_bf16_f32 v72, v72, v73\n"
    "v_cvt_pk_bf16_f32 v73, v74, v75\n"
    "v_mfma_f32_16x16x32_bf16 v[80:83], v[224:227], v[100:103], v[80:83]\n"
    "ds_read_b64 a[254:255], v11 offset:14368\n"
    "ds_read_b64 a[246:247], v11 offset:10528\n"
    "v_mfma_f32_16x16x32_bf16 v[84:87], a[240:243], v[100:103], v[84:87]\n"
    "s_waitcnt lgkmcnt(12)\n"
    "ds_read_b128 v[100:103], v12 offset:1920\n"
    "v_mfma_f32_16x16x32_bf16 v[80:83], v[192:195], v[104:107], v[80:83]\n"
    "ds_read_b64 a[240:241], v11 offset:8448\n"
    "v_mfma_f32_16x16x32_bf16 v[84:87], v[224:227], v[104:107], v[84:87]\n"
    "s_waitcnt lgkmcnt(13)\n"
    "ds_read_b128 v[104:107], v12 offset:2432\n"
    "ds_write_b64 v13, v[72:73] offset:0\n"
    "v_cvt_pk_bf16_f32 v76, v76, v77\n"
    "v_cvt_pk_bf16_f32 v77, v78, v79\n"
    "ds_write_b64 v13, v[76:77] offset:512\n"
    "s_waitcnt vmcnt(0)\n"
    "s_waitcnt lgkmcnt(0)\n"
    "s_barrier\n"
    "v_mov_b32 v68, s64\n"
    "v_mov_b32 v69, s65\n"
    "v_cvt_f32_u32 v70, v0\n"
    "buffer_store_dwordx2 v[68:69], v70, s[52:55], s4 offen offset:0 sc1\n"
    "v_mfma_f32_16x16x32_bf16 v[80:83], v[160:163], v[108:111], v[80:83]\n"
    "ds_read_b64 a[242:243], v11 offset:8480\n"
    "s_lshl1_add_u32 s44, s18, s45\n"
    "v_mfma_f32_16x16x32_bf16 v[84:87], v[192:195], v[108:111], v[84:87]\n"
    "buffer_load_dwordx4 a[48:51], v2, s[52:55], s44 offen offset:0 sc1\n"
    "v_mfma_f32_16x16x32_bf16 v[80:83], v[128:131], v[112:115], v[80:83]\n"
    "ds_read_b128 v[108:111], v12 offset:2944\n"
    "v_mfma_f32_16x16x32_bf16 v[84:87], v[160:163], v[112:115], v[84:87]\n"
    "ds_read_b128 v[112:115], v12 offset:320\n"
    "v_mfma_f32_16x16x32_bf16 v[84:87], v[128:131], v[116:119], v[84:87]\n"
    "s_waitcnt lgkmcnt(2)\n"
    "ds_read_b128 v[116:119], v12 offset:832\n"
    "v_mfma_f32_16x16x32_bf16 v[80:83], a[240:243], v[120:123], v[80:83]\n"
    "ds_read_b64_tr_b16 v[72:73], v14 offset:0\n"
    "v_mfma_f32_16x16x32_bf16 v[84:87], a[244:247], v[120:123], v[84:87]\n"
    "ds_read_b128 v[120:123], v12 offset:1344\n"
    "ds_read_b64 a[246:247], v11 offset:10784\n"
    "v_mfma_f32_16x16x32_bf16 v[80:83], v[228:231], v[124:127], v[80:83]\n"
    "ds_read_b64 a[244:245], v11 offset:10752\n"
    "v_mfma_f32_16x16x32_bf16 v[84:87], a[240:243], v[124:127], v[84:87]\n"
    "ds_read_b128 v[124:127], v12 offset:1856\n"
    "ds_read_b64 a[242:243], v11 offset:8736\n"
    "v_mfma_f32_16x16x32_bf16 v[80:83], v[196:199], v[96:99], v[80:83]\n"
    "ds_read_b64_tr_b16 v[74:75], v14 offset:32\n"
    "v_mfma_f32_16x16x32_bf16 v[84:87], v[228:231], v[96:99], v[84:87]\n"
    "ds_read_b128 v[96:99], v12 offset:2368\n"
    "v_mfma_f32_16x16x32_bf16 v[80:83], v[164:167], v[100:103], v[80:83]\n"
    "ds_read_b64 a[240:241], v11 offset:8704\n"
    "v_mfma_f32_16x16x32_bf16 v[84:87], v[196:199], v[100:103], v[84:87]\n"
    "ds_read_b128 v[100:103], v12 offset:2880\n"
    "v_mfma_f32_16x16x32_bf16 v[80:83], v[132:135], v[104:107], v[80:83]\n"
    "buffer_load_dwordx4 a[52:55], v2, s[52:55], s44 offen offset:1024 sc1\n"
    "v_mfma_f32_16x16x32_bf16 v[84:87], v[164:167], v[104:107], v[84:87]\n"
    "s_waitcnt lgkmcnt(12)\n"
    "ds_read_b128 v[104:107], v12 offset:256\n"
    "v_mfma_f32_16x16x32_bf16 v[84:87], v[132:135], v[108:111], v[84:87]\n"
    "s_waitcnt lgkmcnt(2)\n"
    "ds_read_b128 v[108:111], v12 offset:768\n"
    "v_lshlrev_b32 v76, 16, v72\n"
    "v_and_b32 v77, 0xffff0000, v72\n"
    "v_mfma_f32_16x16x32_bf16 v[80:83], a[240:243], v[112:115], v[80:83]\n"
    "buffer_load_dwordx4 a[56:59], v2, s[52:55], s44 offen offset:2048 sc1\n"
    "v_fmac_f32 v48, v56, v76\n"
    "v_mfma_f32_16x16x32_bf16 v[84:87], a[244:247], v[112:115], v[84:87]\n"
    "v_fmac_f32 v49, v57, v77\n"
    "v_cvt_pk_bf16_f32 v72, v48, v49\n"
    "v_lshlrev_b32 v78, 16, v73\n"
    "v_mfma_f32_16x16x32_bf16 v[80:83], v[232:235], v[116:119], v[80:83]\n"
    "ds_read_b128 v[112:115], v12 offset:1280\n"
    "ds_read_b64 a[244:245], v11 offset:11008\n"
    "v_mfma_f32_16x16x32_bf16 v[84:87], a[240:243], v[116:119], v[84:87]\n"
    "ds_read_b128 v[116:119], v12 offset:1792\n"
    "ds_read_b64 a[242:243], v11 offset:8992\n"
    "v_mfma_f32_16x16x32_bf16 v[80:83], v[200:203], v[120:123], v[80:83]\n"
    "ds_read_b64 a[240:241], v11 offset:8960\n"
    "ds_read_b64 a[246:247], v11 offset:11040\n"
    "v_mfma_f32_16x16x32_bf16 v[84:87], v[232:235], v[120:123], v[84:87]\n"
    "buffer_load_dwordx4 a[60:63], v2, s[52:55], s44 offen offset:3072 sc1\n"
    "v_and_b32 v79, 0xffff0000, v73\n"
    "v_mfma_f32_16x16x32_bf16 v[80:83], v[168:171], v[124:127], v[80:83]\n"
    "ds_read_b128 v[120:123], v12 offset:2304\n"
    "v_fmac_f32 v50, v58, v78\n"
    "v_fmac_f32 v51, v59, v79\n"
    "v_mfma_f32_16x16x32_bf16 v[84:87], v[200:203], v[124:127], v[84:87]\n"
    "ds_read_b128 v[124:127], v12 offset:2816\n"
    "v_cvt_pk_bf16_f32 v73, v50, v51\n"
    "v_lshlrev_b32 v76, 16, v74\n"
    "v_mfma_f32_16x16x32_bf16 v[80:83], v[136:139], v[96:99], v[80:83]\n"
    "s_add_u32 s44, s44, 4096\n"
    "v_and_b32 v77, 0xffff0000, v74\n"
    "v_fmac_f32 v52, v60, v76\n"
    "v_mfma_f32_16x16x32_bf16 v[84:87], v[168:171], v[96:99], v[84:87]\n"
    "s_waitcnt lgkmcnt(10)\n"
    "buffer_load_dwordx4 a[64:67], v2, s[52:55], s44 offen offset:0 sc1\n"
    "v_mfma_f32_16x16x32_bf16 v[84:87], v[136:139], v[100:103], v[84:87]\n"
    "s_waitcnt lgkmcnt(3)\n"
    "ds_read_b128 v[96:99], v12 offset:192\n"
    "ds_read_b128 v[100:103], v12 offset:704\n"
    "v_mfma_f32_16x16x32_bf16 v[80:83], a[240:243], v[104:107], v[80:83]\n"
    "s_waitcnt lgkmcnt(4)\n"
    "v_fmac_f32 v53, v61, v77\n"
    "v_cvt_pk_bf16_f32 v74, v52, v53\n"
    "v_mfma_f32_16x16x32_bf16 v[84:87], a[244:247], v[104:107], v[84:87]\n"
    "ds_read_b128 v[104:107], v12 offset:1216\n"
    "ds_read_b64 a[244:245], v11 offset:11264\n"
    "v_mfma_f32_16x16x32_bf16 v[80:83], v[236:239], v[108:111], v[80:83]\n"
    "buffer_load_dwordx4 a[68:71], v2, s[52:55], s44 offen offset:1024 sc1\n"
    "v_mfma_f32_16x16x32_bf16 v[84:87], a[240:243], v[108:111], v[84:87]\n"
    "ds_read_b128 v[108:111], v12 offset:1728\n"
    "ds_read_b64 a[242:243], v11 offset:9248\n"
    "v_mfma_f32_16x16x32_bf16 v[80:83], v[204:207], v[112:115], v[80:83]\n"
    "ds_read_b64 a[240:241], v11 offset:9216\n"
    "ds_read_b64 a[246:247], v11 offset:11296\n"
    "v_mfma_f32_16x16x32_bf16 v[84:87], v[236:239], v[112:115], v[84:87]\n"
    "ds_read_b128 v[112:115], v12 offset:2240\n"
    "v_lshlrev_b32 v78, 16, v75\n"
    "v_mfma_f32_16x16x32_bf16 v[80:83], v[172:175], v[116:119], v[80:83]\n"
    "s_lshl1_add_u32 s44, s19, s45\n"
    "v_and_b32 v79, 0xffff0000, v75\n"
    "v_fmac_f32 v54, v62, v78\n"
    "v_mfma_f32_16x16x32_bf16 v[84:87], v[204:207], v[116:119], v[84:87]\n"
    "s_waitcnt lgkmcnt(10)\n"
    "ds_read_b128 v[116:119], v12 offset:2752\n"
    "v_fmac_f32 v55, v63, v79\n"
    "v_mfma_f32_16x16x32_bf16 v[80:83], v[140:143], v[120:123], v[80:83]\n"
    "buffer_load_dwordx4 a[72:75], v2, s[52:55], s44 offen offset:0 sc1\n"
    "v_mfma_f32_16x16x32_bf16 v[84:87], v[172:175], v[120:123], v[84:87]\n"
    "s_waitcnt lgkmcnt(10)\n"
    "ds_read_b128 v[120:123], v12 offset:128\n"
    "v_cvt_pk_bf16_f32 v75, v54, v55\n"
    "v_mfma_f32_16x16x32_bf16 v[84:87], v[140:143], v[124:127], v[84:87]\n"
    "s_waitcnt lgkmcnt(4)\n"
    "s_add_u32 s9, s28, 40960\n"
    "v_mfma_f32_16x16x32_bf16 v[80:83], a[240:243], v[96:99], v[80:83]\n"
    "s_waitcnt lgkmcnt(3)\n"
    "ds_read_b128 v[124:127], v12 offset:640\n"
    "v_mfma_f32_16x16x32_bf16 v[84:87], a[244:247], v[96:99], v[84:87]\n"
    "ds_read_b128 v[96:99], v12 offset:1152\n"
    "ds_read_b64 a[246:247], v11 offset:11552\n"
    "v_mfma_f32_16x16x32_bf16 v[80:83], v[240:243], v[100:103], v[80:83]\n"
    "ds_read_b64 a[244:245], v11 offset:11520\n"
    "v_mfma_f32_16x16x32_bf16 v[84:87], a[240:243], v[100:103], v[84:87]\n"
    "buffer_store_dwordx4 v[72:75], v15, s[52:55], s9 offen sc1\n"
    "v_mfma_f32_16x16x32_bf16 v[80:83], v[208:211], v[104:107], v[80:83]\n"
    "ds_read_b128 v[100:103], v12 offset:1664\n"
    "ds_read_b64 a[242:243], v11 offset:9504\n"
    "v_mfma_f32_16x16x32_bf16 v[84:87], v[240:243], v[104:107], v[84:87]\n"
    "ds_read_b128 v[104:107], v12 offset:2176\n"
    "v_accvgpr_read_b32 v64, a233\n"
    "v_accvgpr_read_b32 v65, a216\n"
    "v_mfma_f32_16x16x32_bf16 v[80:83], v[176:179], v[108:111], v[80:83]\n"
    "ds_read_b64 a[240:241], v11 offset:9472\n"
    "v_accvgpr_read_b32 v71, a218\n"
    "v_lshlrev_b32 v48, 16, v40\n"
    "v_mfma_f32_16x16x32_bf16 v[84:87], v[208:211], v[108:111], v[84:87]\n"
    "s_waitcnt lgkmcnt(10)\n"
    "buffer_load_dwordx4 a[76:79], v2, s[52:55], s44 offen offset:1024 sc1\n"
    "v_and_b32 v49, 0xffff0000, v40\n"
    "v_mfma_f32_16x16x32_bf16 v[80:83], v[144:147], v[112:115], v[80:83]\n"
    "ds_read_b128 v[108:111], v12 offset:2688\n"
    "v_lshlrev_b32 v50, 16, v41\n"
    "v_and_b32 v51, 0xffff0000, v41\n"
    "v_mfma_f32_16x16x32_bf16 v[84:87], v[176:179], v[112:115], v[84:87]\n"
    "s_waitcnt lgkmcnt(10)\n"
    "ds_read_b128 v[112:115], v12 offset:64\n"
    "v_lshlrev_b32 v52, 16, v42\n"
    "v_and_b32 v53, 0xffff0000, v42\n"
    "v_mfma_f32_16x16x32_bf16 v[84:87], v[144:147], v[116:119], v[84:87]\n"
    "s_waitcnt lgkmcnt(2)\n"
    "ds_read_b128 v[116:119], v12 offset:576\n"
    "v_lshlrev_b32 v54, 16, v43\n"
    "v_and_b32 v55, 0xffff0000, v43\n"
    "v_mfma_f32_16x16x32_bf16 v[80:83], a[240:243], v[120:123], v[80:83]\n"
    "buffer_load_dwordx4 a[80:83], v2, s[52:55], s44 offen offset:2048 sc1\n"
    "v_fma_f32 v56, v48, v24, v25\n"
    "v_mfma_f32_16x16x32_bf16 v[84:87], a[244:247], v[120:123], v[84:87]\n"
    "ds_read_b128 v[120:123], v12 offset:1088\n"
    "ds_read_b64 a[246:247], v11 offset:11808\n"
    "v_mfma_f32_16x16x32_bf16 v[80:83], v[244:247], v[124:127], v[80:83]\n"
    "ds_read_b64 a[244:245], v11 offset:11776\n"
    "v_fma_f32 v57, v49, v24, v25\n"
    "v_fma_f32 v58, v50, v24, v25\n"
    "v_mfma_f32_16x16x32_bf16 v[84:87], a[240:243], v[124:127], v[84:87]\n"
    "ds_read_b128 v[124:127], v12 offset:1600\n"
    "ds_read_b64 a[242:243], v11 offset:9760\n"
    "v_fma_f32 v59, v51, v24, v25\n"
    "v_mfma_f32_16x16x32_bf16 v[80:83], v[212:215], v[96:99], v[80:83]\n"
    "buffer_load_dwordx4 a[84:87], v2, s[52:55], s44 offen offset:3072 sc1\n"
    "v_fma_f32 v60, v52, v26, v27\n"
    "v_fma_f32 v61, v53, v26, v27\n"
    "v_mfma_f32_16x16x32_bf16 v[84:87], v[244:247], v[96:99], v[84:87]\n"
    "ds_read_b128 v[96:99], v12 offset:2112\n"
    "v_fma_f32 v62, v54, v26, v27\n"
    "v_fma_f32 v63, v55, v26, v27\n"
    "v_mfma_f32_16x16x32_bf16 v[80:83], v[180:183], v[100:103], v[80:83]\n"
    "ds_read_b64 a[240:241], v11 offset:9728\n"
    "s_add_u32 s44, s44, 4096\n"
    "buffer_load_dwordx4 a[88:91], v2, s[52:55], s44 offen offset:0 sc1\n"
    "buffer_load_dwordx4 a[92:95], v2, s[52:55], s44 offen offset:1024 sc1\n"
    "buffer_load_dwordx2 v[4:5], v3, s[52:55], s27 offen sc1\n"
    "v_mfma_f32_16x16x32_bf16 v[84:87], v[212:215], v[100:103], v[84:87]\n"
    "ds_read_b128 v[100:103], v12 offset:2624\n"
    "v_mfma_f32_16x16x32_bf16 v[80:83], v[148:151], v[104:107], v[80:83]\n"
    "v_mfma_f32_16x16x32_bf16 v[84:87], v[180:183], v[104:107], v[84:87]\n"
    "s_waitcnt lgkmcnt(10)\n"
    "ds_read_b128 v[104:107], v12 offset:0\n"
    "v_mfma_f32_16x16x32_bf16 v[84:87], v[148:151], v[108:111], v[84:87]\n"
    "s_waitcnt lgkmcnt(2)\n"
    "ds_read_b128 v[108:111], v12 offset:512\n"
    "v_mfma_f32_16x16x32_bf16 v[80:83], a[240:243], v[112:115], v[80:83]\n"
    "v_mfma_f32_16x16x32_bf16 v[84:87], a[244:247], v[112:115], v[84:87]\n"
    "ds_read_b128 v[112:115], v12 offset:1024\n"
    "ds_read_b64 a[246:247], v11 offset:12064\n"
    "v_mfma_f32_16x16x32_bf16 v[80:83], v[248:251], v[116:119], v[80:83]\n"
    "ds_read_b64 a[244:245], v11 offset:12032\n"
    "v_mfma_f32_16x16x32_bf16 v[84:87], a[240:243], v[116:119], v[84:87]\n"
    "ds_read_b128 v[116:119], v12 offset:1536\n"
    "v_mfma_f32_16x16x32_bf16 v[80:83], v[216:219], v[120:123], v[80:83]\n"
    "ds_read_b64 a[240:241], v11 offset:9984\n"
    "v_mfma_f32_16x16x32_bf16 v[84:87], v[248:251], v[120:123], v[84:87]\n"
    "ds_read_b128 v[120:123], v12 offset:2048\n"
    "v_mfma_f32_16x16x32_bf16 v[80:83], v[184:187], v[124:127], v[80:83]\n"
    "ds_read_b64 a[242:243], v11 offset:10016\n"
    "v_mfma_f32_16x16x32_bf16 v[84:87], v[216:219], v[124:127], v[84:87]\n"
    "ds_read_b128 v[124:127], v12 offset:2560\n"
    "v_mfma_f32_16x16x32_bf16 v[80:83], v[152:155], v[96:99], v[80:83]\n"
    "v_mfma_f32_16x16x32_bf16 v[84:87], v[184:187], v[96:99], v[84:87]\n"
    "s_waitcnt lgkmcnt(10)\n"
    "ds_read_b128 v[96:99], v12 offset:448\n"
    "v_mfma_f32_16x16x32_bf16 v[84:87], v[152:155], v[100:103], v[84:87]\n"
    "s_waitcnt lgkmcnt(2)\n"
    "ds_read_b128 v[100:103], v12 offset:960\n"
    "v_mfma_f32_16x16x32_bf16 v[80:83], a[240:243], v[104:107], v[80:83]\n"
    "v_mfma_f32_16x16x32_bf16 v[84:87], a[244:247], v[104:107], v[84:87]\n"
    "ds_read_b128 v[104:107], v12 offset:1472\n"
    "v_mfma_f32_16x16x32_bf16 v[80:83], v[252:255], v[108:111], v[80:83]\n"
    "ds_read_b64 a[244:245], v11 offset:10240\n"
    "v_mfma_f32_16x16x32_bf16 v[84:87], a[240:243], v[108:111], v[84:87]\n"
    "ds_read_b128 v[108:111], v12 offset:1984\n"
    "v_mfma_f32_16x16x32_bf16 v[80:83], v[220:223], v[112:115], v[80:83]\n"
    "ds_read_b64 a[246:247], v11 offset:10272\n"
    "v_mfma_f32_16x16x32_bf16 v[84:87], v[252:255], v[112:115], v[84:87]\n"
    "ds_read_b128 v[112:115], v12 offset:2496\n"
    "v_mfma_f32_16x16x32_bf16 v[80:83], v[188:191], v[116:119], v[80:83]\n"
    "ds_read_b64 a[240:241], v11 offset:8192\n"
    "v_mfma_f32_16x16x32_bf16 v[84:87], v[220:223], v[116:119], v[84:87]\n"
    "ds_read_b128 v[116:119], v12 offset:3008\n"
    "v_mfma_f32_16x16x32_bf16 v[80:83], v[156:159], v[120:123], v[80:83]\n"
    "ds_read_b64 a[242:243], v11 offset:8224\n"
    "v_mfma_f32_16x16x32_bf16 v[84:87], v[188:191], v[120:123], v[84:87]\n"
    "s_waitcnt lgkmcnt(10)\n"
    "ds_read_b128 v[120:123], v12 offset:3520\n"
    "v_mfma_f32_16x16x32_bf16 v[84:87], v[156:159], v[124:127], v[84:87]\n"
    "s_waitcnt lgkmcnt(10)\n"
    "ds_read_b128 v[124:127], v12 offset:4032\n"
    "v_mfma_f32_16x16x32_bf16 v[88:91], a[248:251], v[96:99], 0\n"
    "v_mfma_f32_16x16x32_bf16 v[92:95], a[252:255], v[96:99], 0\n"
    "s_waitcnt lgkmcnt(6)\n"
    "ds_read_b128 v[96:99], v12 offset:384\n"
    "v_cvt_pk_bf16_f32 v80, v80, v81\n"
    "v_cvt_pk_bf16_f32 v81, v82, v83\n"
    "v_mfma_f32_16x16x32_bf16 v[88:91], a[244:247], v[100:103], v[88:91]\n"
    "ds_read_b64 a[252:253], v11 offset:14592\n"
    "v_mfma_f32_16x16x32_bf16 v[92:95], a[248:251], v[100:103], v[92:95]\n"
    "s_waitcnt lgkmcnt(4)\n"
    "ds_read_b128 v[100:103], v12 offset:896\n"
    "v_mfma_f32_16x16x32_bf16 v[88:91], a[240:243], v[104:107], v[88:91]\n"
    "ds_read_b64 a[248:249], v11 offset:12544\n"
    "v_mfma_f32_16x16x32_bf16 v[92:95], a[244:247], v[104:107], v[92:95]\n"
    "ds_read_b128 v[104:107], v12 offset:1408\n"
    "ds_read_b64 a[244:245], v11 offset:10496\n"
    "ds_write_b64 v13, v[80:81] offset:16512\n"
    "v_cvt_pk_bf16_f32 v84, v84, v85\n"
    "v_cvt_pk_bf16_f32 v85, v86, v87\n"
    "ds_write_b64 v13, v[84:85] offset:17024\n"
    "s_waitcnt vmcnt(0)\n"
    "s_waitcnt lgkmcnt(0)\n"
    "s_barrier\n"
    "v_mov_b32 v68, s64\n"
    "v_mov_b32 v69, s65\n"
    "v_cvt_f32_u32 v70, v0\n"
    "s_add_u32 s98, s4, 131072\n"
    "buffer_store_dwordx2 v[68:69], v70, s[52:55], s98 offen offset:0 sc1\n"
    "v_mfma_f32_16x16x32_bf16 v[88:91], v[224:227], v[108:111], v[88:91]\n"
    "ds_read_b64 a[250:251], v11 offset:12576\n"
    "ds_read_b64 a[246:247], v11 offset:10528\n"
    "v_mfma_f32_16x16x32_bf16 v[92:95], a[240:243], v[108:111], v[92:95]\n"
    "v_cmp_ne_u64 vcc, s[64:65], v[4:5]\n"
    "s_cmp_eq_u64 vcc, 0\n"
    "s_cbranch_scc1 .L7_%=\n"
    "s_mov_b32 s98, 100000\n"
    ".L9_%=:\n"
    "buffer_load_dwordx2 v[4:5], v3, s[52:55], s27 offen sc1\n"
    "s_waitcnt vmcnt(0)\n"
    "v_cmp_ne_u64 vcc, s[64:65], v[4:5]\n"
    "s_cmp_eq_u64 vcc, 0\n"
    "s_cbranch_scc1 .L7_%=\n"
    "s_sub_u32 s98, s98, 1\n"
    "s_cbranch_scc0 .L9_%=\n"
    ".L7_%=:\n"
    "v_mfma_f32_16x16x32_bf16 v[88:91], v[192:195], v[112:115], v[88:91]\n"
    "ds_read_b128 v[108:111], v12 offset:1920\n"
    "ds_read_b64 a[240:241], v11 offset:8448\n"
    "v_mfma_f32_16x16x32_bf16 v[92:95], v[224:227], v[112:115], v[92:95]\n"
    "ds_read_b128 v[112:115], v12 offset:2432\n"
    "ds_read_b64 a[242:243], v11 offset:8480\n"
    "ds_read_b64_tr_b16 v[80:81], v14 offset:16512\n"
    "ds_read_b64_tr_b16 v[82:83], v14 offset:16544\n"
    "v_mfma_f32_16x16x32_bf16 v[88:91], v[160:163], v[116:119], v[88:91]\n"
    "ds_read_b64 a[254:255], v11 offset:14624\n"
    "s_lshl1_add_u32 s44, s20, s45\n"
    "v_mfma_f32_16x16x32_bf16 v[92:95], v[192:195], v[116:119], v[92:95]\n"
    "buffer_load_dwordx4 a[96:99], v2, s[52:55], s44 offen offset:0 sc1\n"
    "v_mfma_f32_16x16x32_bf16 v[88:91], v[128:131], v[120:123], v[88:91]\n"
    "ds_read_b128 v[116:119], v12 offset:2944\n"
    "v_mfma_f32_16x16x32_bf16 v[92:95], v[160:163], v[120:123], v[92:95]\n"
    "ds_read_b128 v[120:123], v12 offset:3456\n"
    "v_mfma_f32_16x16x32_bf16 v[92:95], v[128:131], v[124:127], v[92:95]\n"
    "s_waitcnt lgkmcnt(10)\n"
    "ds_read_b128 v[124:127], v12 offset:3968\n"
    "v_mfma_f32_16x16x32_bf16 v[88:91], a[248:251], v[96:99], v[88:91]\n"
    "s_waitcnt lgkmcnt(3)\n"
    "buffer_load_dwordx2 v[66:67], v64, s[52:55], 0 offen sc1\n"
    "v_lshlrev_b32 v84, 16, v80\n"
    "v_mfma_f32_16x16x32_bf16 v[92:95], a[252:255], v[96:99], v[92:95]\n"
    "ds_read_b128 v[96:99], v12 offset:320\n"
    "ds_read_b64 a[254:255], v11 offset:14880\n"
    "v_mfma_f32_16x16x32_bf16 v[88:91], a[244:247], v[100:103], v[88:91]\n"
    "ds_read_b64 a[252:253], v11 offset:14848\n"
    "v_and_b32 v85, 0xffff0000, v80\n"
    "v_fmac_f32 v48, v56, v84\n"
    "v_mfma_f32_16x16x32_bf16 v[92:95], a[248:251], v[100:103], v[92:95]\n"
    "ds_read_b128 v[100:103], v12 offset:832\n"
    "v_fmac_f32 v49, v57, v85\n"
    "v_cvt_pk_bf16_f32 v80, v48, v49\n"
    "v_mfma_f32_16x16x32_bf16 v[88:91], a[240:243], v[104:107], v[88:91]\n"
    "buffer_load_dwordx4 a[100:103], v2, s[52:55], s44 offen offset:1024 sc1\n"
    "v_lshlrev_b32 v86, 16, v81\n"
    "v_mfma_f32_16x16x32_bf16 v[92:95], a[244:247], v[104:107], v[92:95]\n"
    "ds_read_b128 v[104:107], v12 offset:1344\n"
    "ds_read_b64 a[244:245], v11 offset:10752\n"
    "v_mfma_f32_16x16x32_bf16 v[88:91], v[228:231], v[108:111], v[88:91]\n"
    "ds_read_b64 a[248:249], v11 offset:12800\n"
    "ds_read_b64 a[246:247], v11 offset:10784\n"
    "v_mfma_f32_16x16x32_bf16 v[92:95], a[240:243], v[108:111], v[92:95]\n"
    "ds_read_b128 v[108:111], v12 offset:1856\n"
    "ds_read_b64 a[240:241], v11 offset:8704\n"
    "v_mfma_f32_16x16x32_bf16 v[88:91], v[196:199], v[112:115], v[88:91]\n"
    "buffer_load_dwordx4 a[104:107], v2, s[52:55], s44 offen offset:2048 sc1\n"
    "v_and_b32 v87, 0xffff0000, v81\n"
    "v_mfma_f32_16x16x32_bf16 v[92:95], v[228:231], v[112:115], v[92:95]\n"
    "s_waitcnt lgkmcnt(12)\n"
    "ds_read_b128 v[112:115], v12 offset:2368\n"
    "ds_read_b64 a[242:243], v11 offset:8736\n"
    "v_mfma_f32_16x16x32_bf16 v[88:91], v[164:167], v[116:119], v[88:91]\n"
    "ds_read_b64 a[250:251], v11 offset:12832\n"
    "v_fmac_f32 v50, v58, v86\n"
    "v_fmac_f32 v51, v59, v87\n"
    "v_mfma_f32_16x16x32_bf16 v[92:95], v[196:199], v[116:119], v[92:95]\n"
    "s_waitcnt lgkmcnt(14)\n"
    "ds_read_b128 v[116:119], v12 offset:2880\n"
    "v_cvt_pk_bf16_f32 v81, v50, v51\n"
    "v_lshlrev_b32 v84, 16, v82\n"
    "v_mfma_f32_16x16x32_bf16 v[88:91], v[132:135], v[120:123], v[88:91]\n"
    "buffer_load_dwordx4 a[108:111], v2, s[52:55], s44 offen offset:3072 sc1\n"
    "v_and_b32 v85, 0xffff0000, v82\n"
    "v_mfma_f32_16x16x32_bf16 v[92:95], v[164:167], v[120:123], v[92:95]\n"
    "s_waitcnt lgkmcnt(14)\n"
    "ds_read_b128 v[120:123], v12 offset:3392\n"
    "v_fmac_f32 v52, v60, v84\n"
    "v_fmac_f32 v53, v61, v85\n"
    "v_mfma_f32_16x16x32_bf16 v[92:95], v[132:135], v[124:127], v[92:95]\n"
    "s_waitcnt lgkmcnt(2)\n"
    "ds_read_b128 v[124:127], v12 offset:3904\n"
    "v_cvt_pk_bf16_f32 v82, v52, v53\n"
    "v_lshlrev_b32 v86, 16, v83\n"
    "v_mfma_f32_16x16x32_bf16 v[88:91], a[248:251], v[96:99], v[88:91]\n"
    "s_add_u32 s44, s44, 4096\n"
    "v_and_b32 v87, 0xffff0000, v83\n"
    "v_fmac_f32 v54, v62, v86\n"
    "v_fmac_f32 v55, v63, v87\n"
    "v_mfma_f32_16x16x32_bf16 v[92:95], a[252:255], v[96:99], v[92:95]\n"
    "buffer_load_dwordx4 a[112:115], v2, s[52:55], s44 offen offset:0 sc1\n"
    "v_cvt_pk_bf16_f32 v83, v54, v55\n"
    "v_mfma_f32_16x16x32_bf16 v[88:91], a[244:247], v[100:103], v[88:91]\n"
    "ds_read_b128 v[96:99], v12 offset:256\n"
    "ds_read_b64 a[252:253], v11 offset:15104\n"
    "v_mfma_f32_16x16x32_bf16 v[92:95], a[248:251], v[100:103], v[92:95]\n"
    "ds_read_b128 v[100:103], v12 offset:768\n"
    "ds_read_b64 a[254:255], v11 offset:15136\n"
    "v_mfma_f32_16x16x32_bf16 v[88:91], a[240:243], v[104:107], v[88:91]\n"
    "ds_read_b64 a[248:249], v11 offset:13056\n"
    "s_add_u32 s9, s28, 81920\n"
    "v_mfma_f32_16x16x32_bf16 v[92:95], a[244:247], v[104:107], v[92:95]\n"
    "buffer_store_dwordx4 v[80:83], v15, s[52:55], s9 offen sc1\n"
    "v_mfma_f32_16x16x32_bf16 v[88:91], v[232:235], v[108:111], v[88:91]\n"
    "ds_read_b128 v[104:107], v12 offset:1280\n"
    "ds_read_b64 a[246:247], v11 offset:11040\n"
    "v_mfma_f32_16x16x32_bf16 v[92:95], a[240:243], v[108:111], v[92:95]\n"
    "ds_read_b128 v[108:111], v12 offset:1792\n"
    "ds_read_b64 a[240:241], v11 offset:8960\n"
    "v_mfma_f32_16x16x32_bf16 v[88:91], v[200:203], v[112:115], v[88:91]\n"
    "ds_read_b64 a[250:251], v11 offset:13088\n"
    "ds_read_b64 a[242:243], v11 offset:8992\n"
    "v_mfma_f32_16x16x32_bf16 v[92:95], v[232:235], v[112:115], v[92:95]\n"
    "s_waitcnt lgkmcnt(13)\n"
    "buffer_load_dwordx4 a[116:119], v2, s[52:55], s44 offen offset:1024 sc1\n"
    "v_lshlrev_b32 v48, 16, v44\n"
    "v_mfma_f32_16x16x32_bf16 v[88:91], v[168:171], v[116:119], v[88:91]\n"
    "ds_read_b128 v[112:115], v12 offset:2304\n"
    "v_and_b32 v49, 0xffff0000, v44\n"
    "v_lshlrev_b32 v50, 16, v45\n"
    "v_mfma_f32_16x16x32_bf16 v[92:95], v[200:203], v[116:119], v[92:95]\n"
    "s_waitcnt lgkmcnt(13)\n"
    "ds_read_b128 v[116:119], v12 offset:2816\n"
    "v_and_b32 v51, 0xffff0000, v45\n"
    "v_lshlrev_b32 v52, 16, v46\n"
    "v_mfma_f32_16x16x32_bf16 v[88:91], v[136:139], v[120:123], v[88:91]\n"
    "ds_read_b64 a[244:245], v11 offset:11008\n"
    "v_and_b32 v53, 0xffff0000, v46\n"
    "v_lshlrev_b32 v54, 16, v47\n"
    "v_mfma_f32_16x16x32_bf16 v[92:95], v[168:171], v[120:123], v[92:95]\n"
    "s_waitcnt lgkmcnt(14)\n"
    "s_lshl1_add_u32 s44, s21, s45\n"
    "v_and_b32 v55, 0xffff0000, v47\n"
    "v_fma_f32 v56, v48, v28, v29\n"
    "v_fma_f32 v57, v49, v28, v29\n"
    "v_fma_f32 v58, v50, v28, v29\n"
    "v_mfma_f32_16x16x32_bf16 v[92:95], v[136:139], v[124:127], v[92:95]\n"
    "s_waitcnt lgkmcnt(4)\n"
    "ds_read_b128 v[120:123], v12 offset:3328\n"
    "ds_read_b128 v[124:127], v12 offset:3840\n"
    "v_fma_f32 v59, v51, v28, v29\n"
    "v_fma_f32 v60, v52, v30, v31\n"
    "v_mfma_f32_16x16x32_bf16 v[88:91], a[248:251], v[96:99], v[88:91]\n"
    "s_waitcnt vmcnt(6)\n"
    "v_cmp_ne_u64 vcc, s[64:65], v[66:67]\n"
    "s_cmp_eq_u64 vcc, 0\n"
    "s_cbranch_scc1 .L10_%=\n"
    "s_mov_b32 s98, 100000\n"
    ".L12_%=:\n"
    "buffer_load_dwordx2 v[66:67], v64, s[52:55], 0 offen sc1\n"
    "s_waitcnt vmcnt(0)\n"
    "v_cmp_ne_u64 vcc, s[64:65], v[66:67]\n"
    "s_cmp_eq_u64 vcc, 0\n"
    "s_cbranch_scc1 .L10_%=\n"
    "s_sub_u32 s98, s98, 1\n"
    "s_cbranch_scc0 .L12_%=\n"
    ".L10_%=:\n"
    "v_mfma_f32_16x16x32_bf16 v[92:95], a[252:255], v[96:99], v[92:95]\n"
    "s_waitcnt lgkmcnt(2)\n"
    "ds_read_b128 v[96:99], v12 offset:192\n"
    "ds_read_b64 a[252:253], v11 offset:15360\n"
    "v_fma_f32 v61, v53, v30, v31\n"
    "v_mfma_f32_16x16x32_bf16 v[88:91], a[244:247], v[100:103], v[88:91]\n"
    "buffer_load_dwordx4 a[120:123], v2, s[52:55], s44 offen offset:0 sc1\n"
    "buffer_load_dwordx4 a[124:127], v2, s[52:55], s44 offen offset:1024 sc1\n"
    "v_mfma_f32_16x16x32_bf16 v[92:95], a[248:251], v[100:103], v[92:95]\n"
    "ds_read_b128 v[100:103], v12 offset:704\n"
    "ds_read_b64 a[254:255], v11 offset:15392\n"
    "v_mfma_f32_16x16x32_bf16 v[88:91], a[240:243], v[104:107], v[88:91]\n"
    "ds_read_b64 a[248:249], v11 offset:13312\n"
    "v_fma_f32 v62, v54, v30, v31\n"
    "v_mfma_f32_16x16x32_bf16 v[92:95], a[244:247], v[104:107], v[92:95]\n"
    "ds_read_b128 v[104:107], v12 offset:1216\n"
    "ds_read_b64 a[246:247], v11 offset:11296\n"
    "v_mfma_f32_16x16x32_bf16 v[88:91], v[236:239], v[108:111], v[88:91]\n"
    "buffer_load_dwordx4 v[16:19], v65, s[52:55], s16 offen sc1\n"
    "v_mfma_f32_16x16x32_bf16 v[92:95], a[240:243], v[108:111], v[92:95]\n"
    "ds_read_b128 v[108:111], v12 offset:1728\n"
    "ds_read_b64 a[240:241], v11 offset:9216\n"
    "v_mfma_f32_16x16x32_bf16 v[88:91], v[204:207], v[112:115], v[88:91]\n"
    "ds_read_b64 a[250:251], v11 offset:13344\n"
    "ds_read_b64 a[242:243], v11 offset:9248\n"
    "v_mfma_f32_16x16x32_bf16 v[92:95], v[236:239], v[112:115], v[92:95]\n"
    "ds_read_b128 v[112:115], v12 offset:2240\n"
    "v_fma_f32 v63, v55, v30, v31\n"
    "v_mfma_f32_16x16x32_bf16 v[88:91], v[172:175], v[116:119], v[88:91]\n"
    "buffer_load_dwordx4 v[20:23], v65, s[52:55], s17 offen sc1\n"
    "v_mfma_f32_16x16x32_bf16 v[92:95], v[204:207], v[116:119], v[92:95]\n"
    "s_waitcnt lgkmcnt(13)\n"
    "ds_read_b128 v[116:119], v12 offset:2752\n"
    "v_accvgpr_read_b32 v162, a216\n"
    "v_mfma_f32_16x16x32_bf16 v[88:91], v[140:143], v[120:123], v[88:91]\n"
    "ds_read_b64 a[244:245], v11 offset:11264\n"
    "v_accvgpr_read_b32 v163, a217\n"
    "v_mfma_f32_16x16x32_bf16 v[92:95], v[172:175], v[120:123], v[92:95]\n"
    "s_waitcnt lgkmcnt(14)\n"
    "ds_read_b128 v[120:123], v12 offset:3264\n"
    "v_accvgpr_read_b32 v164, a218\n"
    "v_mfma_f32_16x16x32_bf16 v[92:95], v[140:143], v[124:127], v[92:95]\n"
    "s_waitcnt lgkmcnt(5)\n"
    "buffer_load_dwordx4 v[32:35], v65, s[52:55], s18 offen sc1\n"
    "v_mfma_f32_16x16x32_bf16 v[88:91], a[248:251], v[96:99], v[88:91]\n"
    "ds_read_b128 v[124:127], v12 offset:3776\n"
    "v_accvgpr_read_b32 v165, a219\n"
    "v_mfma_f32_16x16x32_bf16 v[92:95], a[252:255], v[96:99], v[92:95]\n"
    "s_waitcnt lgkmcnt(2)\n"
    "ds_read_b128 v[96:99], v12 offset:128\n"
    "ds_read_b64 a[254:255], v11 offset:15648\n"
    "v_mfma_f32_16x16x32_bf16 v[88:91], a[244:247], v[100:103], v[88:91]\n"
    "ds_read_b64 a[252:253], v11 offset:15616\n"
    "v_accvgpr_read_b32 v166, a220\n"
    "v_mfma_f32_16x16x32_bf16 v[92:95], a[248:251], v[100:103], v[92:95]\n"
    "buffer_load_dwordx4 v[36:39], v65, s[52:55], s19 offen sc1\n"
    "v_mfma_f32_16x16x32_bf16 v[88:91], a[240:243], v[104:107], v[88:91]\n"
    "ds_read_b128 v[100:103], v12 offset:640\n"
    "v_accvgpr_read_b32 v167, a221\n"
    "v_mfma_f32_16x16x32_bf16 v[92:95], a[244:247], v[104:107], v[92:95]\n"
    "ds_read_b128 v[104:107], v12 offset:1152\n"
    "ds_read_b64 a[244:245], v11 offset:11520\n"
    "v_mfma_f32_16x16x32_bf16 v[88:91], v[240:243], v[108:111], v[88:91]\n"
    "ds_read_b64 a[248:249], v11 offset:13568\n"
    "ds_read_b64 a[246:247], v11 offset:11552\n"
    "v_mfma_f32_16x16x32_bf16 v[92:95], a[240:243], v[108:111], v[92:95]\n"
    "buffer_load_dwordx4 v[72:75], v65, s[52:55], s20 offen sc1\n"
    "v_mfma_f32_16x16x32_bf16 v[88:91], v[208:211], v[112:115], v[88:91]\n"
    "ds_read_b128 v[108:111], v12 offset:1664\n"
    "ds_read_b64 a[240:241], v11 offset:9472\n"
    "v_mfma_f32_16x16x32_bf16 v[92:95], v[240:243], v[112:115], v[92:95]\n"
    "ds_read_b128 v[112:115], v12 offset:2176\n"
    "ds_read_b64 a[242:243], v11 offset:9504\n"
    "v_mfma_f32_16x16x32_bf16 v[88:91], v[176:179], v[116:119], v[88:91]\n"
    "ds_read_b64 a[250:251], v11 offset:13600\n"
    "v_accvgpr_read_b32 v168, a222\n"
    "v_mfma_f32_16x16x32_bf16 v[92:95], v[208:211], v[116:119], v[92:95]\n"
    "s_waitcnt lgkmcnt(14)\n"
    "buffer_load_dwordx4 v[76:79], v65, s[52:55], s21 offen sc1\n"
    "v_mfma_f32_16x16x32_bf16 v[88:91], v[144:147], v[120:123], v[88:91]\n"
    "ds_read_b128 v[116:119], v12 offset:2688\n"
    "v_accvgpr_read_b32 v169, a223\n"
    "v_mfma_f32_16x16x32_bf16 v[92:95], v[176:179], v[120:123], v[92:95]\n"
    "s_waitcnt lgkmcnt(14)\n"
    "ds_read_b128 v[120:123], v12 offset:3200\n"
    "v_accvgpr_read_b32 v170, a224\n"
    "v_mfma_f32_16x16x32_bf16 v[92:95], v[144:147], v[124:127], v[92:95]\n"
    "s_waitcnt lgkmcnt(2)\n"
    "ds_read_b128 v[124:127], v12 offset:3712\n"
    "v_accvgpr_read_b32 v171, a225\n"
    "v_mfma_f32_16x16x32_bf16 v[88:91], a[248:251], v[96:99], v[88:91]\n"
    "buffer_load_dwordx4 v[24:27], v65, s[52:55], s22 offen sc1\n"
    "v_mfma_f32_16x16x32_bf16 v[92:95], a[252:255], v[96:99], v[92:95]\n"
    "ds_read_b128 v[96:99], v12 offset:64\n"
    "ds_read_b64 a[254:255], v11 offset:15904\n"
    "v_mfma_f32_16x16x32_bf16 v[88:91], a[244:247], v[100:103], v[88:91]\n"
    "ds_read_b64 a[252:253], v11 offset:15872\n"
    "v_accvgpr_read_b32 v172, a226\n"
    "v_mfma_f32_16x16x32_bf16 v[92:95], a[248:251], v[100:103], v[92:95]\n"
    "ds_read_b128 v[100:103], v12 offset:576\n"
    "v_accvgpr_read_b32 v173, a227\n"
    "v_mfma_f32_16x16x32_bf16 v[88:91], a[240:243], v[104:107], v[88:91]\n"
    "buffer_load_dwordx4 v[40:43], v65, s[52:55], s23 offen sc1\n"
    "v_mfma_f32_16x16x32_bf16 v[92:95], a[244:247], v[104:107], v[92:95]\n"
    "ds_read_b128 v[104:107], v12 offset:1088\n"
    "ds_read_b64 a[244:245], v11 offset:11776\n"
    "v_mfma_f32_16x16x32_bf16 v[88:91], v[244:247], v[108:111], v[88:91]\n"
    "ds_read_b64 a[248:249], v11 offset:13824\n"
    "ds_read_b64 a[246:247], v11 offset:11808\n"
    "v_mfma_f32_16x16x32_bf16 v[92:95], a[240:243], v[108:111], v[92:95]\n"
    "ds_read_b128 v[108:111], v12 offset:1600\n"
    "ds_read_b64 a[240:241], v11 offset:9728\n"
    "v_mfma_f32_16x16x32_bf16 v[88:91], v[212:215], v[112:115], v[88:91]\n"
    "buffer_load_dwordx4 v[80:83], v65, s[52:55], s24 offen sc1\n"
    "v_mfma_f32_16x16x32_bf16 v[92:95], v[244:247], v[112:115], v[92:95]\n"
    "s_waitcnt lgkmcnt(12)\n"
    "ds_read_b128 v[112:115], v12 offset:2112\n"
    "ds_read_b64 a[242:243], v11 offset:9760\n"
    "v_mfma_f32_16x16x32_bf16 v[88:91], v[180:183], v[116:119], v[88:91]\n"
    "ds_read_b64 a[250:251], v11 offset:13856\n"
    "v_accvgpr_read_b32 v174, a228\n"
    "v_mfma_f32_16x16x32_bf16 v[92:95], v[212:215], v[116:119], v[92:95]\n"
    "s_waitcnt lgkmcnt(14)\n"
    "ds_read_b128 v[116:119], v12 offset:2624\n"
    "v_accvgpr_read_b32 v175, a229\n"
    "v_mfma_f32_16x16x32_bf16 v[88:91], v[148:151], v[120:123], v[88:91]\n"
    "buffer_load_dwordx4 v[84:87], v65, s[52:55], s25 offen sc1\n"
    "v_mfma_f32_16x16x32_bf16 v[92:95], v[180:183], v[120:123], v[92:95]\n"
    "s_waitcnt lgkmcnt(14)\n"
    "ds_read_b128 v[120:123], v12 offset:3136\n"
    "v_accvgpr_read_b32 v176, a230\n"
    "v_mfma_f32_16x16x32_bf16 v[92:95], v[148:151], v[124:127], v[92:95]\n"
    "s_waitcnt lgkmcnt(2)\n"
    "ds_read_b128 v[124:127], v12 offset:3648\n"
    "v_accvgpr_read_b32 v177, a231\n"
    "v_mfma_f32_16x16x32_bf16 v[88:91], a[248:251], v[96:99], v[88:91]\n"
    "buffer_load_dwordx4 a[128:131], v2, s[52:55], s44 offen offset:2048 sc1\n"
    "v_mfma_f32_16x16x32_bf16 v[92:95], a[252:255], v[96:99], v[92:95]\n"
    "v_accvgpr_read_b32 v178, a232\n"
    "v_accvgpr_read_b32 v179, a233\n"
    "v_mfma_f32_16x16x32_bf16 v[88:91], a[244:247], v[100:103], v[88:91]\n"
    "ds_read_b128 v[96:99], v12 offset:0\n"
    "ds_read_b64 a[252:253], v11 offset:16128\n"
    "v_mfma_f32_16x16x32_bf16 v[92:95], a[248:251], v[100:103], v[92:95]\n"
    "ds_read_b128 v[100:103], v12 offset:512\n"
    "ds_read_b64 a[254:255], v11 offset:16160\n"
    "v_mfma_f32_16x16x32_bf16 v[88:91], a[240:243], v[104:107], v[88:91]\n"
    "ds_read_b64 a[248:249], v11 offset:14080\n"
    "v_mov_b32 v144, 0x3f803f80\n"
    "v_mfma_f32_16x16x32_bf16 v[92:95], a[244:247], v[104:107], v[92:95]\n"
    "buffer_load_dwordx4 a[132:135], v2, s[52:55], s44 offen offset:3072 sc1\n"
    "v_mfma_f32_16x16x32_bf16 v[88:91], v[248:251], v[108:111], v[88:91]\n"
    "ds_read_b128 v[104:107], v12 offset:1024\n"
    "ds_read_b64 a[246:247], v11 offset:12064\n"
    "v_mfma_f32_16x16x32_bf16 v[92:95], a[240:243], v[108:111], v[92:95]\n"
    "ds_read_b128 v[108:111], v12 offset:1536\n"
    "ds_read_b64 a[240:241], v11 offset:9984\n"
    "v_mfma_f32_16x16x32_bf16 v[88:91], v[216:219], v[112:115], v[88:91]\n"
    "ds_read_b64 a[250:251], v11 offset:14112\n"
    "ds_read_b64 a[242:243], v11 offset:10016\n"
    "v_mfma_f32_16x16x32_bf16 v[92:95], v[248:251], v[112:115], v[92:95]\n"
    "s_waitcnt lgkmcnt(13)\n"
    "s_add_u32 s44, s44, 4096\n"
    "v_mov_b32 v145, 0x3f803f80\n"
    "v_mov_b32 v146, 0x3f803f80\n"
    "v_mfma_f32_16x16x32_bf16 v[88:91], v[184:187], v[116:119], v[88:91]\n"
    "ds_read_b128 v[112:115], v12 offset:2048\n"
    "v_mov_b32 v147, 0x3f803f80\n"
    "v_mfma_f32_16x16x32_bf16 v[92:95], v[216:219], v[116:119], v[92:95]\n"
    "s_waitcnt lgkmcnt(13)\n"
    "ds_read_b128 v[116:119], v12 offset:2560\n"
    "v_add_u32 v162, 40960, v162\n"
    "v_mfma_f32_16x16x32_bf16 v[88:91], v[152:155], v[120:123], v[88:91]\n"
    "ds_read_b64 a[244:245], v11 offset:12032\n"
    "v_mfma_f32_16x16x32_bf16 v[92:95], v[184:187], v[120:123], v[92:95]\n"
    "s_waitcnt lgkmcnt(14)\n"
    "buffer_load_dwordx4 a[136:139], v2, s[52:55], s44 offen offset:0 sc1\n"
    "v_mfma_f32_16x16x32_bf16 v[92:95], v[152:155], v[124:127], v[92:95]\n"
    "s_waitcnt lgkmcnt(4)\n"
    "ds_read_b128 v[120:123], v12 offset:3072\n"
    "ds_read_b128 v[124:127], v12 offset:3584\n"
    "v_mfma_f32_16x16x32_bf16 v[88:91], a[248:251], v[96:99], v[88:91]\n"
    "s_waitcnt vmcnt(12)\n"
    "ds_write2st64_b64 v71, v[16:17], v[18:19] offset0:80 offset1:82\n"
    "v_mfma_f32_16x16x32_bf16 v[92:95], a[252:255], v[96:99], v[92:95]\n"
    "s_waitcnt lgkmcnt(3)\n"
    "buffer_load_dwordx4 a[140:143], v2, s[52:55], s44 offen offset:1024 sc1\n"
    "v_mfma_f32_16x16x32_bf16 v[88:91], a[244:247], v[100:103], v[88:91]\n"
    "s_lshl1_add_u32 s44, s22, s45\n"
    "v_mfma_f32_16x16x32_bf16 v[92:95], a[248:251], v[100:103], v[92:95]\n"
    "s_waitcnt vmcnt(12)\n"
    "ds_write2st64_b64 v71, v[20:21], v[22:23] offset0:88 offset1:90\n"
    "v_mfma_f32_16x16x32_bf16 v[88:91], a[240:243], v[104:107], v[88:91]\n"
    "buffer_load_dwordx4 a[144:147], v2, s[52:55], s44 offen offset:0 sc1\n"
    "v_mfma_f32_16x16x32_bf16 v[92:95], a[244:247], v[104:107], v[92:95]\n"
    "v_mfma_f32_16x16x32_bf16 v[88:91], v[252:255], v[108:111], v[88:91]\n"
    "s_waitcnt vmcnt(12)\n"
    "ds_write2st64_b64 v71, v[32:33], v[34:35] offset0:96 offset1:98\n"
    "v_mfma_f32_16x16x32_bf16 v[92:95], a[240:243], v[108:111], v[92:95]\n"
    "buffer_load_dwordx4 a[148:151], v2, s[52:55], s44 offen offset:1024 sc1\n"
    "v_mfma_f32_16x16x32_bf16 v[88:91], v[220:223], v[112:115], v[88:91]\n"
    "v_mfma_f32_16x16x32_bf16 v[92:95], v[252:255], v[112:115], v[92:95]\n"
    "s_waitcnt vmcnt(12)\n"
    "ds_write2st64_b64 v71, v[36:37], v[38:39] offset0:104 offset1:106\n"
    "v_mfma_f32_16x16x32_bf16 v[88:91], v[188:191], v[116:119], v[88:91]\n"
    "buffer_load_dwordx4 a[152:155], v2, s[52:55], s44 offen offset:2048 sc1\n"
    "v_mfma_f32_16x16x32_bf16 v[92:95], v[220:223], v[116:119], v[92:95]\n"
    "s_waitcnt lgkmcnt(5)\n"
    "v_mfma_f32_16x16x32_bf16 v[88:91], v[156:159], v[120:123], v[88:91]\n"
    "s_waitcnt vmcnt(12)\n"
    "ds_write2st64_b64 v71, v[72:73], v[74:75] offset0:112 offset1:114\n"
    "v_mfma_f32_16x16x32_bf16 v[92:95], v[188:191], v[120:123], v[92:95]\n"
    "s_waitcnt lgkmcnt(5)\n"
    "buffer_load_dwordx4 a[156:159], v2, s[52:55], s44 offen offset:3072 sc1\n"
    "v_mfma_f32_16x16x32_bf16 v[92:95], v[156:159], v[124:127], v[92:95]\n"
    "s_nop 1\n"
    "v_cvt_pk_bf16_f32 v88, v88, v89\n"
    "v_cvt_pk_bf16_f32 v89, v90, v91\n"
    "ds_write_b64 v13, v[88:89] offset:0\n"
    "s_nop 2\n"
    "v_cvt_pk_bf16_f32 v92, v92, v93\n"
    "v_cvt_pk_bf16_f32 v93, v94, v95\n"
    "ds_write_b64 v13, v[92:93] offset:512\n"
    "s_waitcnt lgkmcnt(0)\n"
    "s_barrier\n"
    "ds_read_b64_tr_b16 v[88:89], v14 offset:0\n"
    "ds_read_b64_tr_b16 v[90:91], v14 offset:32\n"
    "s_waitcnt lgkmcnt(1)\n"
    "v_lshlrev_b32 v92, 16, v88\n"
    "v_and_b32 v93, 0xffff0000, v88\n"
    "v_fmac_f32 v48, v56, v92\n"
    "s_add_u32 s44, s44, 4096\n"
    "buffer_load_dwordx4 a[160:163], v2, s[52:55], s44 offen offset:0 sc1\n"
    "v_fmac_f32 v49, v57, v93\n"
    "v_cvt_pk_bf16_f32 v88, v48, v49\n"
    "v_lshlrev_b32 v94, 16, v89\n"
    "buffer_load_dwordx4 a[164:167], v2, s[52:55], s44 offen offset:1024 sc1\n"
    "v_and_b32 v95, 0xffff0000, v89\n"
    "v_fmac_f32 v50, v58, v94\n"
    "v_fmac_f32 v51, v59, v95\n"
    "s_lshl1_add_u32 s44, s23, s45\n"
    "buffer_load_dwordx4 a[168:171], v2, s[52:55], s44 offen offset:0 sc1\n"
    "v_cvt_pk_bf16_f32 v89, v50, v51\n"
    "s_waitcnt lgkmcnt(0)\n"
    "v_lshlrev_b32 v92, 16, v90\n"
    "v_and_b32 v93, 0xffff0000, v90\n"
    "buffer_load_dwordx4 a[172:175], v2, s[52:55], s44 offen offset:1024 sc1\n"
    "v_fmac_f32 v52, v60, v92\n"
    "v_fmac_f32 v53, v61, v93\n"
    "v_cvt_pk_bf16_f32 v90, v52, v53\n"
    "buffer_load_dwordx4 a[176:179], v2, s[52:55], s44 offen offset:2048 sc1\n"
    "v_lshlrev_b32 v94, 16, v91\n"
    "v_and_b32 v95, 0xffff0000, v91\n"
    "v_fmac_f32 v54, v62, v94\n"
    "buffer_load_dwordx4 a[180:183], v2, s[52:55], s44 offen offset:3072 sc1\n"
    "v_fmac_f32 v55, v63, v95\n"
    "v_cvt_pk_bf16_f32 v91, v54, v55\n"
    "s_add_u32 s9, s28, 122880\n"
    "buffer_store_dwordx4 v[88:91], v15, s[52:55], s9 offen sc1\n"
    "s_add_u32 s44, s44, 4096\n"
    "buffer_load_dwordx4 a[184:187], v2, s[52:55], s44 offen offset:0 sc1\n"
    "s_waitcnt vmcnt(20)\n"
    "ds_write2st64_b64 v71, v[76:77], v[78:79] offset0:120 offset1:122\n"
    "s_waitcnt vmcnt(19)\n"
    "ds_write2st64_b64 v71, v[24:25], v[26:27] offset0:128 offset1:130\n"
    "s_waitcnt vmcnt(18)\n"
    "ds_write2st64_b64 v71, v[40:41], v[42:43] offset0:136 offset1:138\n"
    "buffer_load_dwordx4 a[188:191], v2, s[52:55], s44 offen offset:1024 sc1\n"
    "s_waitcnt vmcnt(18)\n"
    "ds_write2st64_b64 v71, v[80:81], v[82:83] offset0:144 offset1:146\n"
    "s_waitcnt vmcnt(17)\n"
    "ds_write2st64_b64 v71, v[84:85], v[86:87] offset0:152 offset1:154\n"
    "s_lshl1_add_u32 s44, s24, s45\n"
    "buffer_load_dwordx4 a[192:195], v2, s[52:55], s44 offen offset:0 sc1\n"
    "buffer_load_dwordx4 a[196:199], v2, s[52:55], s44 offen offset:1024 sc1\n"
    "buffer_load_dwordx4 a[200:203], v2, s[52:55], s44 offen offset:2048 sc1\n"
    "buffer_load_dwordx4 a[204:207], v2, s[52:55], s44 offen offset:3072 sc1\n"
    "s_add_u32 s44, s44, 4096\n"
    "buffer_load_dwordx4 a[208:211], v2, s[52:55], s44 offen offset:0 sc1\n"
    "buffer_load_dwordx4 a[212:215], v2, s[52:55], s44 offen offset:1024 sc1\n"
    "s_branch .J1_%=\n"
    ".SP_%=:\n"
    "v_lshrrev_b32 v2, 6, v0\n"
    "s_nop 0\n"
    "v_readfirstlane_b32 s3, v2\n"
    "v_lshrrev_b32 v16, 3, v1\n"
    "v_and_b32 v17, 3, v16\n"
    "v_lshrrev_b32 v18, 5, v1\n"
    "v_and_b32 v19, 6, v16\n"
    "v_xor_b32 v19, v19, v1\n"
    "v_and_b32 v19, 7, v19\n"
    "v_lshl_add_u32 v20, v18, 3, v17\n"
    "v_mul_u32_u24 v20, 3072, v20\n"
    "v_lshl_add_u32 v2, v19, 4, v20\n"
    "v_xor_b32 v3, 16, v2\n"
    "v_lshl_add_u32 v20, v18, 4, v17\n"
    "v_mul_u32_u24 v20, 1280, v20\n"
    "v_lshlrev_b32 v21, 1, v17\n"
    "v_sub_u32 v21, v1, v21\n"
    "v_and_b32 v21, 7, v21\n"
    "v_lshl_add_u32 v4, v21, 4, v20\n"
    "s_sub_u32 s8, s2, 160\n"
    "s_lshl3_add_u32 s66, s8, s3\n"
    "s_mul_i32 s66, s66, 5120\n"
    "s_and_b32 s6, s3, 1\n"
    "s_lshr_b32 s7, s3, 1\n"
    "s_lshl2_add_u32 s6, s6, s7\n"
    "s_and_b32 s7, s2, 3\n"
    "s_lshl4_add_u32 s6, s6, s7\n"
    "s_mul_i32 s13, s6, 768\n"
    "s_bfe_u32 s6, s2, 0x30002\n"
    "s_mul_i32 s67, s6, 98304\n"
    "s_add_u32 s67, s67, s13\n"
    "s_lshl_b32 s15, s3, 10\n"
    "s_cmp_lt_u32 s2, 224\n"
    "s_waitcnt lgkmcnt(0)\n"
    "s_nop 0\n"
    "s_cbranch_scc1 .L13_%=\n"
    "s_add_u32 m0, s15, 73728\n"
    "s_nop 0\n"
    "buffer_load_dwordx4 v2, s[40:43], s67 offen offset:0 nt lds\n"
    "s_add_u32 m0, m0, 24576\n"
    "s_nop 0\n"
    "buffer_load_dwordx4 v3, s[44:47], s67 offen offset:0 nt lds\n"
    "s_sub_u32 m0, m0, 20608\n"
    "s_nop 0\n"
    "buffer_load_dwordx4 v2, s[40:43], s67 offen offset:128 nt lds\n"
    "s_add_u32 m0, m0, 24576\n"
    "s_nop 0\n"
    "buffer_load_dwordx4 v3, s[44:47], s67 offen offset:128 nt lds\n"
    "s_sub_u32 m0, m0, 20608\n"
    "s_nop 0\n"
    "buffer_load_dwordx4 v2, s[40:43], s67 offen offset:256 nt lds\n"
    "s_add_u32 m0, m0, 24576\n"
    "s_nop 0\n"
    "buffer_load_dwordx4 v3, s[44:47], s67 offen offset:256 nt lds\n"
    "s_sub_u32 m0, m0, 20608\n"
    "s_nop 0\n"
    "buffer_load_dwordx4 v2, s[40:43], s67 offen offset:384 nt lds\n"
    "s_add_u32 m0, m0, 24576\n"
    "s_nop 0\n"
    "buffer_load_dwordx4 v3, s[44:47], s67 offen offset:384 nt lds\n"
    "s_sub_u32 m0, m0, 20608\n"
    "s_nop 0\n"
    "buffer_load_dwordx4 v2, s[40:43], s67 offen offset:512 nt lds\n"
    "s_add_u32 m0, m0, 24576\n"
    "s_nop 0\n"
    "buffer_load_dwordx4 v3, s[44:47], s67 offen offset:512 nt lds\n"
    "s_sub_u32 m0, m0, 20608\n"
    "s_nop 0\n"
    "buffer_load_dwordx4 v2, s[40:43], s67 offen offset:640 nt lds\n"
    "s_add_u32 m0, m0, 24576\n"
    "s_nop 0\n"
    "buffer_load_dwordx4 v3, s[44:47], s67 offen offset:640 nt lds\n"
    "s_branch .L14_%=\n"
    ".L13_%=:\n"
    "s_mul_i32 m0, s3, 10240\n"
    "s_nop 0\n"
    "buffer_load_dwordx4 v4, s[16:19], s66 offen offset:0 nt lds\n"
    "s_add_u32 m0, m0, 896\n"
    "s_nop 0\n"
    "buffer_load_dwordx4 v4, s[16:19], s66 offen offset:128 nt lds\n"
    "s_add_u32 m0, m0, 896\n"
    "s_nop 0\n"
    "buffer_load_dwordx4 v4, s[16:19], s66 offen offset:256 nt lds\n"
    "s_add_u32 m0, m0, 896\n"
    "s_nop 0\n"
    "buffer_load_dwordx4 v4, s[16:19], s66 offen offset:384 nt lds\n"
    "s_add_u32 m0, m0, 896\n"
    "s_nop 0\n"
    "buffer_load_dwordx4 v4, s[16:19], s66 offen offset:512 nt lds\n"
    "s_add_u32 m0, m0, 896\n"
    "s_nop 0\n"
    "buffer_load_dwordx4 v4, s[16:19], s66 offen offset:640 nt lds\n"
    "s_add_u32 m0, m0, 896\n"
    "s_nop 0\n"
    "buffer_load_dwordx4 v4, s[16:19], s66 offen offset:768 nt lds\n"
    "s_add_u32 m0, m0, 896\n"
    "s_nop 0\n"
    "buffer_load_dwordx4 v4, s[16:19], s66 offen offset:896 nt lds\n"
    "s_add_u32 m0, m0, 896\n"
    "s_nop 0\n"
    "buffer_load_dwordx4 v4, s[16:19], s66 offen offset:1024 nt lds\n"
    "s_add_u32 m0, m0, 896\n"
    "s_nop 0\n"
    "buffer_load_dwordx4 v4, s[16:19], s66 offen offset:1152 nt lds\n"
    ".L14_%=:\n"
    "buffer_load_dwordx2 v[12:13], off, s[52:55], 0 sc1\n"
    "s_and_b32 s8, s2, 3\n"
    "s_lshl_b32 s4, s2, 7\n"
    "s_add_u32 s4, s4, 36864\n"
    "s_bfe_u32 s12, s2, 0x20003\n"
    "s_lshl2_add_u32 s12, s8, s12\n"
    "s_bfe_u32 s8, s2, 0x10002\n"
    "s_lshr_b32 s11, s2, 5\n"
    "s_lshl3_add_u32 s11, s8, s11\n"
    "s_lshl4_add_u32 s5, s11, s12\n"
    "s_lshl_b32 s5, s5, 7\n"
    "s_add_u32 s5, s5, 69632\n"
    "s_lshr_b32 s14, s3, 1\n"
    "v_min_u32 v68, 5, v1\n"
    "v_lshrrev_b32 v69, 1, v68\n"
    "v_and_b32 v68, 1, v68\n"
    "v_lshlrev_b32 v69, 17, v69\n"
    "v_lshl_add_u32 v68, v68, 15, v69\n"
    "s_sub_u32 s8, s4, 32768\n"
    "buffer_load_dword v70, v68, s[52:55], s8 offen sc1\n"
    "buffer_load_dword v71, v69, s[52:55], s5 offen sc1\n"
    "v_mul_u32_u24 v20, 24, v18\n"
    "v_add_u32 v20, v20, v17\n"
    "v_mul_u32_u24 v20, 1280, v20\n"
    "v_lshl_add_u32 v5, v19, 4, v20\n"
    "v_lshlrev_b32 v14, 4, v1\n"
    "s_and_b32 s8, s2, 3\n"
    "s_lshl_b32 s8, s8, 7\n"
    "s_add_u32 s8, s8, 32768\n"
    "v_and_b32 v9, 7, v1\n"
    "v_lshl_add_u32 v9, v9, 9, s8\n"
    "s_and_b32 s6, s2, 16\n"
    "s_lshl_b32 s6, s6, 5\n"
    "s_add_u32 s8, s8, s6\n"
    "s_sub_u32 s8, s8, 8192\n"
    "v_min_u32 v15, 11, v1\n"
    "v_lshl_add_u32 v15, v15, 10, s8\n"
    "v_bfe_u32 v16, v1, 2, 3\n"
    "v_lshlrev_b32 v17, 10, v18\n"
    "v_lshl_add_u32 v17, v16, 7, v17\n"
    "v_lshrrev_b32 v16, 1, v16\n"
    "v_xor_b32 v20, s3, v16\n"
    "v_lshl_add_u32 v20, v20, 5, v17\n"
    "v_and_b32 v21, 3, v1\n"
    "v_lshl_add_u32 v20, v21, 3, v20\n"
    "v_add_u32 v6, 49152, v20\n"
    "v_bfe_u32 v18, v1, 1, 1\n"
    "v_lshlrev_b32 v16, 5, v16\n"
    "v_lshl_add_u32 v7, v18, 4, v16\n"
    "v_mul_u32_u24 v20, 24576, v18\n"
    "v_add_u32 v8, v17, v20\n"
    "v_add_u32 v8, 73728, v8\n"
    "v_and_b32 v21, 1, v1\n"
    "v_lshlrev_b32 v18, 3, v18\n"
    "v_lshl_add_u32 v18, v21, 4, v18\n"
    "s_mul_i32 s8, s3, 96\n"
    "v_add_u32 v18, s8, v18\n"
    "v_lshl_add_u32 v21, v21, 3, s8\n"
    "v_add_u32 v16, 0, v18\n"
    "v_lshrrev_b32 v17, 7, v16\n"
    "v_and_b32 v16, 127, v16\n"
    "v_lshl_add_u32 v17, v17, 12, v8\n"
    "v_xad_u32 v72, v16, v7, v17\n"
    "v_add_u32 v16, 32, v21\n"
    "v_lshrrev_b32 v17, 7, v16\n"
    "v_and_b32 v16, 127, v16\n"
    "v_lshl_add_u32 v17, v17, 12, v8\n"
    "v_xad_u32 v74, v16, v7, v17\n"
    "v_xor_b32 v73, 8, v72\n"
    "v_add_u32 v16, 48, v18\n"
    "v_lshrrev_b32 v17, 7, v16\n"
    "v_and_b32 v16, 127, v16\n"
    "v_lshl_add_u32 v17, v17, 12, v8\n"
    "v_xad_u32 v75, v16, v7, v17\n"
    "v_add_u32 v16, 80, v21\n"
    "v_lshrrev_b32 v17, 7, v16\n"
    "v_and_b32 v16, 127, v16\n"
    "v_lshl_add_u32 v17, v17, 12, v8\n"
    "v_xad_u32 v77, v16, v7, v17\n"
    "v_xor_b32 v76, 8, v75\n"
    "v_and_b32 v17, 3, v1\n"
    "v_lshrrev_b32 v18, 2, v1\n"
    "v_lshl_add_u32 v19, v17, 2, v18\n"
    "v_and_b32 v19, 15, v19\n"
    "v_lshlrev_b32 v19, 3, v19\n"
    "v_lshl_add_u32 v19, v17, 7, v19\n"
    "s_mul_i32 s8, s3, 10240\n"
    "v_add_u32 v22, s8, v19\n"
    "s_sub_u32 s8, s2, 160\n"
    "s_lshl2_add_u32 s8, s8, s3\n"
    "s_lshl_b32 s8, s8, 6\n"
    "v_lshl_add_u32 v20, v17, 4, s8\n"
    "v_mul_u32_u24 v18, 32896, v18\n"
    "v_add_u32 v23, v18, v20\n"
    "v_lshrrev_b32 v18, 5, v1\n"
    "v_lshl_add_u32 v20, v18, 3, v20\n"
    "v_and_b32 v86, 15, v1\n"
    "v_lshrrev_b32 v87, 4, v1\n"
    "v_lshlrev_b32 v165, 4, v1\n"
    "s_mul_i32 s8, s11, 163840\n"
    "s_add_u32 s8, s8, 5767168\n"
    "v_lshl_add_u32 v162, v0, 4, s8\n"
    "s_and_b32 s6, s3, 1\n"
    "s_lshl_b32 s8, s14, 11\n"
    "s_lshl_b32 s7, s6, 9\n"
    "s_add_u32 s8, s8, s7\n"
    "s_add_u32 s8, s8, 81920\n"
    "v_lshrrev_b32 v84, 1, v87\n"
    "v_lshl_add_u32 v85, v84, 8, s8\n"
    "v_lshl_add_u32 v85, v86, 4, v85\n"
    "v_and_b32 v164, 1, v87\n"
    "v_xor_b32 v84, s6, v84\n"
    "v_mul_u32_u24 v176, 49152, v84\n"
    "s_lshl_b32 s8, s3, 8\n"
    "v_lshl_add_u32 v175, v86, 4, s8\n"
    "v_add_u32 v176, v176, v175\n"
    "v_lshl_add_u32 v177, v164, 3, v176\n"
    "v_lshl_add_u32 v176, v164, 10, v176\n"
    "v_lshl_add_u32 v164, v164, 3, v85\n"
    "v_lshlrev_b32 v84, 8, v84\n"
    "v_lshl_add_u32 v84, v86, 4, v84\n"
    "v_add_u32 v175, 61440, v84\n"
    "s_lshl_b32 s8, s6, 8\n"
    "s_lshl3_add_u32 s8, s14, s8\n"
    "s_add_u32 s8, s8, 61440\n"
    "v_lshl_add_u32 v84, v86, 4, s8\n"
    "v_lshlrev_b32 v85, 3, v1\n"
    "v_add_u32 v85, 61952, v85\n"
    "v_lshrrev_b32 v88, 2, v86\n"
    "v_cmp_eq_u32 vcc, v88, v87\n"
    "s_nop 1\n"
    "v_cndmask_b32 v174, v85, v84, vcc\n"
    "s_lshl_b32 s7, s14, 11\n"
    "s_xor_b32 s8, s6, 0\n"
    "s_lshl_b32 s8, s8, 10\n"
    "s_add_u32 s8, s8, 122880\n"
    "v_add_u32 v84, s8, v165\n"
    "v_add_u32 v166, s7, v84\n"
    "v_subrev_u32 v167, s7, v84\n"
    "s_xor_b32 s8, s6, 1\n"
    "s_lshl_b32 s8, s8, 10\n"
    "s_add_u32 s8, s8, 122880\n"
    "v_add_u32 v84, s8, v165\n"
    "v_add_u32 v168, s7, v84\n"
    "v_subrev_u32 v169, s7, v84\n"
    "v_subrev_u32 v170, 40960, v166\n"
    "v_subrev_u32 v171, 40960, v167\n"
    "v_subrev_u32 v172, 40960, v168\n"
    "v_subrev_u32 v173, 40960, v169\n"
    "s_mul_i32 s8, s3, 24\n"
    "s_bfe_u32 s7, s2, 0x10005\n"
    "s_add_u32 s8, s8, s7\n"
    "s_lshl_b32 s8, s8, 7\n"
    "s_add_u32 s8, s8, 57344\n"
    "v_min_u32 v84, 11, v1\n"
    "v_lshl_add_u32 v163, v84, 8, s8\n"
    "v_lshlrev_b32 v178, 4, v0\n"
    "v_min_u32 v179, 39, v1\n"
    "s_lshl_b32 s9, s3, 9\n"
    "s_add_u32 s9, s9, 36864\n"
    "v_and_b32 v84, 3, v179\n"
    "v_lshrrev_b32 v179, 2, v179\n"
    "v_lshl_add_u32 v84, v84, 7, s9\n"
    "v_lshl_add_u32 v179, v179, 11, v84\n"
    "s_mov_b32 s32, 0x22222222\n"
    "s_mov_b32 s33, 0x22222222\n"
    "s_mov_b32 s34, 0x44444444\n"
    "s_mov_b32 s35, 0x44444444\n"
    "s_mov_b32 s36, 0x88888888\n"
    "s_mov_b32 s37, 0x88888888\n"
    "s_mul_i32 s38, s11, 393216\n"
    "s_mul_i32 s8, s12, 3072\n"
    "s_add_u32 s38, s38, s8\n"
    "s_add_u32 s38, s38, 8388608\n"
    "s_bfe_u32 s8, s2, 0x30003\n"
    "s_lshl2_add_u32 s8, s8, s3\n"
    "s_mul_i32 s39, s8, 61440\n"
    "s_add_u32 s39, s39, 14680064\n"
    "v_mov_b32 v144, 0x3f803f80\n"
    "v_mov_b32 v145, 0x3f803f80\n"
    "v_mov_b32 v146, 0x3f803f80\n"
    "v_mov_b32 v147, 0x3f803f80\n"
    "v_and_b32 v16, 15, v1\n"
    "v_cmp_eq_u32 vcc, 0, v16\n"
    "s_nop 1\n"
    "v_cndmask_b32 v96, 0, v144, vcc\n"
    "v_cndmask_b32 v97, 0, v144, vcc\n"
    "v_cndmask_b32 v98, 0, v144, vcc\n"
    "v_cndmask_b32 v99, 0, v144, vcc\n"
    "v_cmp_eq_u32 vcc, 1, v16\n"
    "s_nop 1\n"
    "v_cndmask_b32 v100, 0, v144, vcc\n"
    "v_cndmask_b32 v101, 0, v144, vcc\n"
    "v_cndmask_b32 v102, 0, v144, vcc\n"
    "v_cndmask_b32 v103, 0, v144, vcc\n"
    "v_cmp_eq_u32 vcc, 2, v16\n"
    "s_nop 1\n"
    "v_cndmask_b32 v104, 0, v144, vcc\n"
    "v_cndmask_b32 v105, 0, v144, vcc\n"
    "v_cndmask_b32 v106, 0, v144, vcc\n"
    "v_cndmask_b32 v107, 0, v144, vcc\n"
    "v_cmp_eq_u32 vcc, 3, v16\n"
    "s_nop 1\n"
    "v_cndmask_b32 v108, 0, v144, vcc\n"
    "v_cndmask_b32 v109, 0, v144, vcc\n"
    "v_cndmask_b32 v110, 0, v144, vcc\n"
    "v_cndmask_b32 v111, 0, v144, vcc\n"
    "v_cmp_eq_u32 vcc, 4, v16\n"
    "s_nop 1\n"
    "v_cndmask_b32 v112, 0, v144, vcc\n"
    "v_cndmask_b32 v113, 0, v144, vcc\n"
    "v_cndmask_b32 v114, 0, v144, vcc\n"
    "v_cndmask_b32 v115, 0, v144, vcc\n"
    "v_cmp_eq_u32 vcc, 5, v16\n"
    "s_nop 1\n"
    "v_cndmask_b32 v116, 0, v144, vcc\n"
    "v_cndmask_b32 v117, 0, v144, vcc\n"
    "v_cndmask_b32 v118, 0, v144, vcc\n"
    "v_cndmask_b32 v119, 0, v144, vcc\n"
    "v_cmp_eq_u32 vcc, 6, v16\n"
    "s_nop 1\n"
    "v_cndmask_b32 v120, 0, v144, vcc\n"
    "v_cndmask_b32 v121, 0, v144, vcc\n"
    "v_cndmask_b32 v122, 0, v144, vcc\n"
    "v_cndmask_b32 v123, 0, v144, vcc\n"
    "v_cmp_eq_u32 vcc, 7, v16\n"
    "s_nop 1\n"
    "v_cndmask_b32 v124, 0, v144, vcc\n"
    "v_cndmask_b32 v125, 0, v144, vcc\n"
    "v_cndmask_b32 v126, 0, v144, vcc\n"
    "v_cndmask_b32 v127, 0, v144, vcc\n"
    "v_cmp_eq_u32 vcc, 8, v16\n"
    "s_nop 1\n"
    "v_cndmask_b32 v128, 0, v144, vcc\n"
    "v_cndmask_b32 v129, 0, v144, vcc\n"
    "v_cndmask_b32 v130, 0, v144, vcc\n"
    "v_cndmask_b32 v131, 0, v144, vcc\n"
    "v_cmp_eq_u32 vcc, 9, v16\n"
    "s_nop 1\n"
    "v_cndmask_b32 v132, 0, v144, vcc\n"
    "v_cndmask_b32 v133, 0, v144, vcc\n"
    "v_cndmask_b32 v134, 0, v144, vcc\n"
    "v_cndmask_b32 v135, 0, v144, vcc\n"
    "v_cmp_eq_u32 vcc, 10, v16\n"
    "s_nop 1\n"
    "v_cndmask_b32 v136, 0, v144, vcc\n"
    "v_cndmask_b32 v137, 0, v144, vcc\n"
    "v_cndmask_b32 v138, 0, v144, vcc\n"
    "v_cndmask_b32 v139, 0, v144, vcc\n"
    "v_cmp_eq_u32 vcc, 11, v16\n"
    "s_nop 1\n"
    "v_cndmask_b32 v140, 0, v144, vcc\n"
    "v_cndmask_b32 v141, 0, v144, vcc\n"
    "v_cndmask_b32 v142, 0, v144, vcc\n"
    "v_cndmask_b32 v143, 0, v144, vcc\n"
    "v_mul_u32_u24 v17, 11, v16\n"
    "v_lshrrev_b32 v17, 5, v17\n"
    "v_mul_u32_u24 v18, 3, v17\n"
    "v_sub_u32 v18, v16, v18\n"
    "v_and_b32 v19, 2, v17\n"
    "v_mad_u32_u24 v17, v19, 3, v17\n"
    "v_mul_u32_u24 v17, 81920, v17\n"
    "v_lshl_add_u32 v17, v18, 6, v17\n"
    "v_and_b32 v18, 48, v1\n"
    "v_add_u32 v17, v17, v18\n"
    "v_add_u32 v17, 6144, v17\n"
    "v_bfrev_b32 v18, 1\n"
    "v_cmp_gt_u32 vcc, 6, v16\n"
    "s_nop 1\n"
    "v_cndmask_b32 v78, v18, v17, vcc\n"
    "v_cmp_gt_u32 vcc, 12, v16\n"
    "s_nop 1\n"
    "v_cndmask_b32 v79, v18, v17, vcc\n"
    "s_lshr_b32 s7, s2, 3\n"
    "s_sub_u32 s7, s7, 12\n"
    "s_and_b32 s6, s2, 3\n"
    "s_lshl3_add_u32 s6, s6, s3\n"
    "s_mul_i32 s61, s6, 163840\n"
    "s_lshr_b32 s6, s7, 1\n"
    "s_lshl_b32 s6, s6, 13\n"
    "s_add_u32 s61, s61, s6\n"
    "s_xor_b32 s6, s7, s3\n"
    "s_and_b32 s6, s6, 1\n"
    "s_add_u32 s61, s61, 16646144\n"
    "s_lshl_b32 s26, s6, 8\n"
    "s_add_u32 s26, s26, s61\n"
    "s_mul_i32 s6, s6, 3072\n"
    "s_add_u32 s61, s61, s6\n"
    "s_mul_i32 s60, s7, 98304\n"
    "s_add_u32 s60, s60, s13\n"
    "s_bfe_u32 s6, s2, 0x10002\n"
    "s_mul_i32 s7, s6, 384\n"
    "s_add_u32 s60, s60, s7\n"
    "s_mul_i32 s7, s6, 655360\n"
    "s_add_u32 s61, s61, s7\n"
    "s_add_u32 s26, s26, s7\n"
    "s_lshr_b32 s63, s2, 1\n"
    "s_sub_u32 s63, s63, 80\n"
    "s_mul_i32 s6, s63, 43\n"
    "s_lshr_b32 s6, s6, 7\n"
    "s_mul_i32 s6, s6, 72\n"
    "s_lshl3_add_u32 s6, s63, s6\n"
    "s_and_b32 s7, s3, 1\n"
    "s_mul_i32 s7, s7, 48\n"
    "s_add_u32 s6, s6, s7\n"
    "s_lshl2_add_u32 s6, s14, s6\n"
    "s_and_b32 s7, s2, 1\n"
    "s_lshl1_add_u32 s6, s6, s7\n"
    "s_mul_i32 s62, s6, 640\n"
    "s_mul_i32 s63, s63, 5120\n"
    "s_mul_i32 s7, s7, 983040\n"
    "s_add_u32 s63, s63, s7\n"
    "s_add_u32 s63, s63, s15\n"
    "s_add_u32 s63, s63, 14680064\n"
    "s_cmp_lt_u32 s2, 224\n"
    "s_nop 0\n"
    "s_cbranch_scc1 .L15_%=\n"
    "s_bfe_u32 s7, s2, 0x30002\n"
    "s_and_b32 s6, s2, 3\n"
    "s_lshl3_add_u32 s6, s6, s3\n"
    "s_mul_i32 s67, s6, 163840\n"
    "s_lshr_b32 s6, s7, 1\n"
    "s_lshl_b32 s6, s6, 13\n"
    "s_add_u32 s67, s67, s6\n"
    "s_xor_b32 s6, s7, s3\n"
    "s_and_b32 s6, s6, 1\n"
    "s_add_u32 s67, s67, 16646144\n"
    "s_lshl_b32 s66, s6, 8\n"
    "s_add_u32 s66, s66, s67\n"
    "s_mul_i32 s6, s6, 3072\n"
    "s_add_u32 s67, s67, s6\n"
    "s_waitcnt vmcnt(0)\n"
    "s_barrier\n"
    "ds_read_b64_tr_b16 v[24:25], v72 offset:0\n"
    "ds_read_b64_tr_b16 v[26:27], v72 offset:2048\n"
    "ds_read_b64_tr_b16 v[28:29], v73 offset:0\n"
    "ds_read_b64_tr_b16 v[30:31], v73 offset:2048\n"
    "ds_read_b64_tr_b16 v[32:33], v74 offset:0\n"
    "ds_read_b64_tr_b16 v[34:35], v74 offset:2048\n"
    "ds_read_b64_tr_b16 v[36:37], v75 offset:0\n"
    "ds_read_b64_tr_b16 v[38:39], v75 offset:2048\n"
    "ds_read_b64_tr_b16 v[40:41], v76 offset:0\n"
    "ds_read_b64_tr_b16 v[42:43], v76 offset:2048\n"
    "ds_read_b64_tr_b16 v[44:45], v77 offset:0\n"
    "ds_read_b64_tr_b16 v[46:47], v77 offset:2048\n"
    "ds_read_b64_tr_b16 v[48:49], v72 offset:12288\n"
    "ds_read_b64_tr_b16 v[50:51], v72 offset:14336\n"
    "ds_read_b64_tr_b16 v[52:53], v73 offset:12288\n"
    "ds_read_b64_tr_b16 v[54:55], v73 offset:14336\n"
    "ds_read_b64_tr_b16 v[56:57], v74 offset:12288\n"
    "ds_read_b64_tr_b16 v[58:59], v74 offset:14336\n"
    "ds_read_b64_tr_b16 v[60:61], v75 offset:12288\n"
    "ds_read_b64_tr_b16 v[62:63], v75 offset:14336\n"
    "ds_read_b64_tr_b16 v[64:65], v76 offset:12288\n"
    "ds_read_b64_tr_b16 v[66:67], v76 offset:14336\n"
    "ds_read_b64_tr_b16 v[68:69], v77 offset:12288\n"
    "ds_read_b64_tr_b16 v[70:71], v77 offset:14336\n"
    "s_waitcnt lgkmcnt(15)\n"
    "buffer_store_dwordx4 v[24:27], v14, s[52:55], s67 offen offset:0 sc1\n"
    "s_waitcnt lgkmcnt(15)\n"
    "buffer_store_dwordx4 v[28:31], v14, s[52:55], s67 offen offset:1024 sc1\n"
    "s_waitcnt lgkmcnt(15)\n"
    "buffer_store_dwordx4 v[32:35], v14, s[52:55], s67 offen offset:2048 sc1\n"
    "v_mfma_f32_16x16x32_bf16 v[80:83], v[24:27], v[96:99], 0\n"
    "v_mfma_f32_16x16x32_bf16 v[80:83], v[28:31], v[100:103], v[80:83]\n"
    "v_mfma_f32_16x16x32_bf16 v[80:83], v[32:35], v[104:107], v[80:83]\n"
    "s_add_u32 s8, s67, 81920\n"
    "s_waitcnt lgkmcnt(15)\n"
    "buffer_store_dwordx4 v[36:39], v14, s[52:55], s8 offen offset:0 sc1\n"
    "s_waitcnt lgkmcnt(14)\n"
    "buffer_store_dwordx4 v[40:43], v14, s[52:55], s8 offen offset:1024 sc1\n"
    "s_waitcnt lgkmcnt(12)\n"
    "buffer_store_dwordx4 v[44:47], v14, s[52:55], s8 offen offset:2048 sc1\n"
    "v_mfma_f32_16x16x32_bf16 v[80:83], v[36:39], v[108:111], v[80:83]\n"
    "v_mfma_f32_16x16x32_bf16 v[80:83], v[40:43], v[112:115], v[80:83]\n"
    "v_mfma_f32_16x16x32_bf16 v[80:83], v[44:47], v[116:119], v[80:83]\n"
    "s_add_u32 s8, s67, 655360\n"
    "s_waitcnt lgkmcnt(10)\n"
    "buffer_store_dwordx4 v[48:51], v14, s[52:55], s8 offen offset:0 sc1\n"
    "s_waitcnt lgkmcnt(8)\n"
    "buffer_store_dwordx4 v[52:55], v14, s[52:55], s8 offen offset:1024 sc1\n"
    "s_waitcnt lgkmcnt(6)\n"
    "buffer_store_dwordx4 v[56:59], v14, s[52:55], s8 offen offset:2048 sc1\n"
    "v_mfma_f32_16x16x32_bf16 v[80:83], v[48:51], v[120:123], v[80:83]\n"
    "v_mfma_f32_16x16x32_bf16 v[80:83], v[52:55], v[124:127], v[80:83]\n"
    "v_mfma_f32_16x16x32_bf16 v[80:83], v[56:59], v[128:131], v[80:83]\n"
    "s_add_u32 s8, s67, 737280\n"
    "s_waitcnt lgkmcnt(4)\n"
    "buffer_store_dwordx4 v[60:63], v14, s[52:55], s8 offen offset:0 sc1\n"
    "s_waitcnt lgkmcnt(2)\n"
    "buffer_store_dwordx4 v[64:67], v14, s[52:55], s8 offen offset:1024 sc1\n"
    "s_waitcnt lgkmcnt(0)\n"
    "buffer_store_dwordx4 v[68:71], v14, s[52:55], s8 offen offset:2048 sc1\n"
    "v_mfma_f32_16x16x32_bf16 v[80:83], v[60:63], v[132:135], v[80:83]\n"
    "v_mfma_f32_16x16x32_bf16 v[80:83], v[64:67], v[136:139], v[80:83]\n"
    "v_mfma_f32_16x16x32_bf16 v[80:83], v[68:71], v[140:143], v[80:83]\n"
    "s_nop 7\n"
    "buffer_store_dwordx4 v[80:83], v79, s[52:55], s66 offen sc1\n"
    "s_nop 1\n"
    "s_branch .L16_%=\n"
    ".L15_%=:\n"
    "s_mov_b32 s6, 0x3f803f80\n"
    "s_waitcnt vmcnt(12)\n"
    "ds_read2st64_b64 v[24:27], v22 offset0:0 offset1:1\n"
    "s_waitcnt vmcnt(11)\n"
    "ds_read2st64_b64 v[28:31], v22 offset0:2 offset1:3\n"
    "s_mov_b32 s8, 409600\n"
    "s_waitcnt lgkmcnt(1)\n"
    "buffer_store_dwordx4 v[24:27], v23, s[52:55], s8 offen sc1\n"
    "v_dot2_f32_bf16 v16, v24, s6, 0\n"
    "v_dot2_f32_bf16 v18, v24, v24, 0\n"
    "v_dot2_f32_bf16 v16, v25, s6, v16\n"
    "v_dot2_f32_bf16 v18, v25, v25, v18\n"
    "v_dot2_f32_bf16 v17, v26, s6, 0\n"
    "v_dot2_f32_bf16 v19, v26, v26, 0\n"
    "v_dot2_f32_bf16 v17, v27, s6, v17\n"
    "v_dot2_f32_bf16 v19, v27, v27, v19\n"
    "s_waitcnt vmcnt(11)\n"
    "ds_read2st64_b64 v[32:35], v22 offset0:4 offset1:5\n"
    "s_mov_b32 s8, 935936\n"
    "s_waitcnt lgkmcnt(1)\n"
    "buffer_store_dwordx4 v[28:31], v23, s[52:55], s8 offen sc1\n"
    "v_dot2_f32_bf16 v16, v28, s6, v16\n"
    "v_dot2_f32_bf16 v18, v28, v28, v18\n"
    "v_dot2_f32_bf16 v16, v29, s6, v16\n"
    "v_dot2_f32_bf16 v18, v29, v29, v18\n"
    "v_dot2_f32_bf16 v17, v30, s6, v17\n"
    "v_dot2_f32_bf16 v19, v30, v30, v19\n"
    "v_dot2_f32_bf16 v17, v31, s6, v17\n"
    "v_dot2_f32_bf16 v19, v31, v31, v19\n"
    "s_waitcnt vmcnt(11)\n"
    "ds_read2st64_b64 v[36:39], v22 offset0:6 offset1:7\n"
    "s_mov_b32 s8, 1462272\n"
    "s_waitcnt lgkmcnt(1)\n"
    "buffer_store_dwordx4 v[32:35], v23, s[52:55], s8 offen sc1\n"
    "v_dot2_f32_bf16 v16, v32, s6, v16\n"
    "v_dot2_f32_bf16 v18, v32, v32, v18\n"
    "v_dot2_f32_bf16 v16, v33, s6, v16\n"
    "v_dot2_f32_bf16 v18, v33, v33, v18\n"
    "v_dot2_f32_bf16 v17, v34, s6, v17\n"
    "v_dot2_f32_bf16 v19, v34, v34, v19\n"
    "v_dot2_f32_bf16 v17, v35, s6, v17\n"
    "v_dot2_f32_bf16 v19, v35, v35, v19\n"
    "s_waitcnt vmcnt(11)\n"
    "ds_read2st64_b64 v[40:43], v22 offset0:8 offset1:9\n"
    "s_mov_b32 s8, 1988608\n"
    "s_waitcnt lgkmcnt(1)\n"
    "buffer_store_dwordx4 v[36:39], v23, s[52:55], s8 offen sc1\n"
    "v_dot2_f32_bf16 v16, v36, s6, v16\n"
    "v_dot2_f32_bf16 v18, v36, v36, v18\n"
    "v_dot2_f32_bf16 v16, v37, s6, v16\n"
    "v_dot2_f32_bf16 v18, v37, v37, v18\n"
    "v_dot2_f32_bf16 v17, v38, s6, v17\n"
    "v_dot2_f32_bf16 v19, v38, v38, v19\n"
    "v_dot2_f32_bf16 v17, v39, s6, v17\n"
    "v_dot2_f32_bf16 v19, v39, v39, v19\n"
    "s_waitcnt vmcnt(11)\n"
    "ds_read2st64_b64 v[44:47], v22 offset0:10 offset1:11\n"
    "s_mov_b32 s8, 2514944\n"
    "s_waitcnt lgkmcnt(1)\n"
    "buffer_store_dwordx4 v[40:43], v23, s[52:55], s8 offen sc1\n"
    "v_dot2_f32_bf16 v16, v40, s6, v16\n"
    "v_dot2_f32_bf16 v18, v40, v40, v18\n"
    "v_dot2_f32_bf16 v16, v41, s6, v16\n"
    "v_dot2_f32_bf16 v18, v41, v41, v18\n"
    "v_dot2_f32_bf16 v17, v42, s6, v17\n"
    "v_dot2_f32_bf16 v19, v42, v42, v19\n"
    "v_dot2_f32_bf16 v17, v43, s6, v17\n"
    "v_dot2_f32_bf16 v19, v43, v43, v19\n"
    "s_waitcnt vmcnt(11)\n"
    "ds_read2st64_b64 v[48:51], v22 offset0:12 offset1:13\n"
    "s_mov_b32 s8, 3041280\n"
    "s_waitcnt lgkmcnt(1)\n"
    "buffer_store_dwordx4 v[44:47], v23, s[52:55], s8 offen sc1\n"
    "v_dot2_f32_bf16 v16, v44, s6, v16\n"
    "v_dot2_f32_bf16 v18, v44, v44, v18\n"
    "v_dot2_f32_bf16 v16, v45, s6, v16\n"
    "v_dot2_f32_bf16 v18, v45, v45, v18\n"
    "v_dot2_f32_bf16 v17, v46, s6, v17\n"
    "v_dot2_f32_bf16 v19, v46, v46, v19\n"
    "v_dot2_f32_bf16 v17, v47, s6, v17\n"
    "v_dot2_f32_bf16 v19, v47, v47, v19\n"
    "s_waitcnt vmcnt(11)\n"
    "ds_read2st64_b64 v[52:55], v22 offset0:14 offset1:15\n"
    "s_mov_b32 s8, 3567616\n"
    "s_waitcnt lgkmcnt(1)\n"
    "buffer_store_dwordx4 v[48:51], v23, s[52:55], s8 offen sc1\n"
    "v_dot2_f32_bf16 v16, v48, s6, v16\n"
    "v_dot2_f32_bf16 v18, v48, v48, v18\n"
    "v_dot2_f32_bf16 v16, v49, s6, v16\n"
    "v_dot2_f32_bf16 v18, v49, v49, v18\n"
    "v_dot2_f32_bf16 v17, v50, s6, v17\n"
    "v_dot2_f32_bf16 v19, v50, v50, v19\n"
    "v_dot2_f32_bf16 v17, v51, s6, v17\n"
    "v_dot2_f32_bf16 v19, v51, v51, v19\n"
    "s_waitcnt vmcnt(11)\n"
    "ds_read2st64_b64 v[56:59], v22 offset0:16 offset1:17\n"
    "s_mov_b32 s8, 4093952\n"
    "s_waitcnt lgkmcnt(1)\n"
    "buffer_store_dwordx4 v[52:55], v23, s[52:55], s8 offen sc1\n"
    "v_dot2_f32_bf16 v16, v52, s6, v16\n"
    "v_dot2_f32_bf16 v18, v52, v52, v18\n"
    "v_dot2_f32_bf16 v16, v53, s6, v16\n"
    "v_dot2_f32_bf16 v18, v53, v53, v18\n"
    "v_dot2_f32_bf16 v17, v54, s6, v17\n"
    "v_dot2_f32_bf16 v19, v54, v54, v19\n"
    "v_dot2_f32_bf16 v17, v55, s6, v17\n"
    "v_dot2_f32_bf16 v19, v55, v55, v19\n"
    "s_waitcnt vmcnt(11)\n"
    "ds_read2st64_b64 v[60:63], v22 offset0:18 offset1:19\n"
    "s_mov_b32 s8, 4620288\n"
    "s_waitcnt lgkmcnt(1)\n"
    "buffer_store_dwordx4 v[56:59], v23, s[52:55], s8 offen sc1\n"
    "v_dot2_f32_bf16 v16, v56, s6, v16\n"
    "v_dot2_f32_bf16 v18, v56, v56, v18\n"
    "v_dot2_f32_bf16 v16, v57, s6, v16\n"
    "v_dot2_f32_bf16 v18, v57, v57, v18\n"
    "v_dot2_f32_bf16 v17, v58, s6, v17\n"
    "v_dot2_f32_bf16 v19, v58, v58, v19\n"
    "v_dot2_f32_bf16 v17, v59, s6, v17\n"
    "v_dot2_f32_bf16 v19, v59, v59, v19\n"
    "s_mov_b32 s8, 5146624\n"
    "s_waitcnt lgkmcnt(0)\n"
    "buffer_store_dwordx4 v[60:63], v23, s[52:55], s8 offen sc1\n"
    "v_dot2_f32_bf16 v16, v60, s6, v16\n"
    "v_dot2_f32_bf16 v18, v60, v60, v18\n"
    "v_dot2_f32_bf16 v16, v61, s6, v16\n"
    "v_dot2_f32_bf16 v18, v61, v61, v18\n"
    "v_dot2_f32_bf16 v17, v62, s6, v17\n"
    "v_dot2_f32_bf16 v19, v62, v62, v19\n"
    "v_dot2_f32_bf16 v17, v63, s6, v17\n"
    "v_dot2_f32_bf16 v19, v63, v63, v19\n"
    "s_nop 1\n"
    "v_permlane32_swap_b32 v16, v17\n"
    "v_permlane32_swap_b32 v18, v19\n"
    "v_add_f32 v16, v16, v17\n"
    "v_add_f32 v18, v18, v19\n"
    "v_mov_b32 v17, v16\n"
    "v_mov_b32 v19, v18\n"
    "s_nop 0\n"
    "v_permlane16_swap_b32 v16, v17\n"
    "v_permlane16_swap_b32 v18, v19\n"
    "v_add_f32 v16, v16, v17\n"
    "v_add_f32 v18, v18, v19\n"
    "s_nop 0\n"
    "v_add_f32_dpp v16, v16, v16 row_ror:4 row_mask:0xf bank_mask:0xf\n"
    "v_add_f32_dpp v18, v18, v18 row_ror:4 row_mask:0xf bank_mask:0xf\n"
    "s_nop 0\n"
    "v_add_f32_dpp v16, v16, v16 row_ror:8 row_mask:0xf bank_mask:0xf\n"
    "v_add_f32_dpp v18, v18, v18 row_ror:8 row_mask:0xf bank_mask:0xf\n"
    "v_mul_f32 v16, 0x3acccccd, v16\n"
    "v_mul_f32 v17, v16, v16\n"
    "s_mov_b32 s6, 0x3acccccd\n"
    "v_fma_f32 v18, v18, s6, -v17\n"
    "v_max_f32 v18, 0, v18\n"
    "v_cvt_pk_bf16_f32 v16, v16, v18\n"
    "v_and_b32 v18, 0xffff0000, v16\n"
    "v_lshlrev_b32 v16, 16, v16\n"
    "v_add_f32 v18, 0x3727c5ac, v18\n"
    "v_cvt_pk_bf16_f32 v18, v18, v18\n"
    "v_and_b32 v18, 0xffff0000, v18\n"
    "v_sqrt_f32 v18, v18\n"
    "s_nop 0\n"
    "v_cvt_pk_bf16_f32 v18, v18, v18\n"
    "v_and_b32 v18, 0xffff0000, v18\n"
    "v_rcp_f32 v18, v18\n"
    "s_nop 0\n"
    "v_mul_f32 v19, v16, v18\n"
    "v_xor_b32 v19, 0x80000000, v19\n"
    "s_mov_b32 s6, 15\n"
    "s_mov_b32 s7, 15\n"
    "s_mov_b32 s8, 102400\n"
    "s_mov_b64 exec, s[6:7]\n"
    "buffer_store_dwordx2 v[18:19], v20, s[52:55], s8 offen sc1\n"
    "s_mov_b64 exec, -1\n"
    "s_nop 0\n"
    ".L16_%=:\n"
    "s_waitcnt vmcnt(0) lgkmcnt(0)\n"
    "v_readfirstlane_b32 s64, v12\n"
    "v_readfirstlane_b32 s65, v13\n"
    "s_add_u32 s64, s64, 0x7f4a7c15\n"
    "s_addc_u32 s65, s65, 0x9e3779b9\n"
    "s_barrier\n"
    "v_mov_b32 v16, s64\n"
    "v_mov_b32 v17, s65\n"
    "v_cvt_f32_u32 v18, v0\n"
    "s_add_u32 s98, s4, 4294934528\n"
    "buffer_store_dwordx2 v[16:17], v18, s[52:55], s98 offen offset:0 sc1\n"
    "s_add_u32 m0, s15, 73728\n"
    "s_nop 0\n"
    "buffer_load_dwordx4 v2, s[40:43], s60 offen offset:0 nt lds\n"
    "s_add_u32 m0, m0, 24576\n"
    "s_nop 0\n"
    "buffer_load_dwordx4 v3, s[44:47], s60 offen offset:0 nt lds\n"
    "s_sub_u32 m0, m0, 20608\n"
    "s_nop 0\n"
    "buffer_load_dwordx4 v2, s[40:43], s60 offen offset:128 nt lds\n"
    "s_add_u32 m0, m0, 24576\n"
    "s_nop 0\n"
    "buffer_load_dwordx4 v3, s[44:47], s60 offen offset:128 nt lds\n"
    "s_sub_u32 m0, m0, 20608\n"
    "s_nop 0\n"
    "buffer_load_dwordx4 v2, s[40:43], s60 offen offset:256 nt lds\n"
    "s_add_u32 m0, m0, 24576\n"
    "s_nop 0\n"
    "buffer_load_dwordx4 v3, s[44:47], s60 offen offset:256 nt lds\n"
    "s_add_u32 m0, s15, 49152\n"
    "s_nop 0\n"
    "buffer_load_dwordx4 v5, s[48:51], s62 offen offset:0 nt lds\n"
    "s_add_u32 m0, m0, 3968\n"
    "s_nop 0\n"
    "buffer_load_dwordx4 v5, s[48:51], s62 offen offset:128 nt lds\n"
    "s_add_u32 m0, m0, 3968\n"
    "s_nop 0\n"
    "buffer_load_dwordx4 v5, s[48:51], s62 offen offset:256 nt lds\n"
    "s_add_u32 m0, m0, 3968\n"
    "s_nop 0\n"
    "buffer_load_dwordx4 v5, s[48:51], s62 offen offset:384 nt lds\n"
    "s_add_u32 m0, m0, 3968\n"
    "s_nop 0\n"
    "buffer_load_dwordx4 v5, s[48:51], s62 offen offset:512 nt lds\n"
    "s_lshr_b32 s8, s2, 5\n"
    "s_lshl_b32 s8, s8, 2\n"
    "s_bitcmp1_b32 s2, 2\n"
    "s_mov_b32 s6, 0x00013210\n"
    "s_cselect_b32 s6, 0x22231032, s6\n"
    "s_mov_b32 s7, 0x42042531\n"
    "s_cselect_b32 s7, 0x53151420, s7\n"
    "s_lshr_b32 s6, s6, s8\n"
    "s_lshr_b32 s7, s7, s8\n"
    "s_lshl_b32 s6, s6, 12\n"
    "s_and_b32 s7, s7, 15\n"
    "s_and_b32 s16, s6, 0x3000\n"
    "s_add_u32 s6, s6, 4096\n"
    "s_and_b32 s17, s6, 0x3000\n"
    "s_add_u32 s6, s6, 4096\n"
    "s_and_b32 s18, s6, 0x3000\n"
    "s_add_u32 s6, s6, 4096\n"
    "s_and_b32 s19, s6, 0x3000\n"
    "s_lshl_b32 s7, s7, 12\n"
    "s_add_u32 s20, s7, 16384\n"
    "s_add_u32 s21, s7, 20480\n"
    "s_sub_u32 s8, s21, 24576\n"
    "s_cmp_lt_u32 s7, 20480\n"
    "s_cselect_b32 s21, s21, s8\n"
    "s_add_u32 s22, s7, 24576\n"
    "s_sub_u32 s8, s22, 24576\n"
    "s_cmp_lt_u32 s7, 16384\n"
    "s_cselect_b32 s22, s22, s8\n"
    "s_add_u32 s23, s7, 28672\n"
    "s_sub_u32 s8, s23, 24576\n"
    "s_cmp_lt_u32 s7, 12288\n"
    "s_cselect_b32 s23, s23, s8\n"
    "s_add_u32 s24, s7, 32768\n"
    "s_sub_u32 s8, s24, 24576\n"
    "s_cmp_lt_u32 s7, 8192\n"
    "s_cselect_b32 s24, s24, s8\n"
    "s_add_u32 s25, s7, 36864\n"
    "s_sub_u32 s8, s25, 24576\n"
    "s_cmp_lt_u32 s7, 4096\n"
    "s_cselect_b32 s25, s25, s8\n"
    "s_lshl2_add_u32 s8, s12, s3\n"
    "s_mul_i32 s45, s8, 81920\n"
    "s_add_u32 s45, s45, 16646144\n"
    "s_mov_b32 s27, 131072\n"
    "s_mov_b32 s29, 262144\n"
    "s_mul_i32 s40, s3, 5120\n"
    "s_bfe_u32 s43, s2, 0x30003\n"
    "s_bfe_u32 s46, s2, 0x10006\n"
    "s_lshr_b32 s8, s2, 7\n"
    "s_and_b32 s7, s2, 7\n"
    "s_lshl1_add_u32 s47, s7, s8\n"
    "s_lshl2_add_u32 s8, s47, s3\n"
    "s_lshl_b32 s41, s8, 9\n"
    "s_add_u32 s41, s41, 69632\n"
    "s_mul_i32 s8, s43, 160\n"
    "s_lshl1_add_u32 s42, s47, s46\n"
    "s_mul_i32 s42, s42, 20480\n"
    "s_add_u32 s42, s42, s8\n"
    "s_mul_i32 s30, s47, 393216\n"
    "s_mul_i32 s8, s46, 49152\n"
    "s_add_u32 s30, s30, s8\n"
    "s_mul_i32 s8, s3, 12288\n"
    "s_add_u32 s30, s30, s8\n"
    "s_add_u32 s30, s30, 8388608\n"
    "s_mul_i32 s31, s47, 163840\n"
    "s_mul_i32 s8, s43, 5120\n"
    "s_add_u32 s31, s31, s8\n"
    "s_lshl3_add_u32 s31, s46, s31\n"
    "s_add_u32 s31, s31, 5767168\n"
    "s_waitcnt vmcnt(5)\n"
    "s_barrier\n"
    "ds_read_b64_tr_b16 v[24:25], v72 offset:0\n"
    "ds_read_b64_tr_b16 v[26:27], v72 offset:2048\n"
    "ds_read_b64_tr_b16 v[28:29], v73 offset:0\n"
    "ds_read_b64_tr_b16 v[30:31], v73 offset:2048\n"
    "ds_read_b64_tr_b16 v[32:33], v74 offset:0\n"
    "ds_read_b64_tr_b16 v[34:35], v74 offset:2048\n"
    "ds_read_b64_tr_b16 v[36:37], v75 offset:0\n"
    "ds_read_b64_tr_b16 v[38:39], v75 offset:2048\n"
    "ds_read_b64_tr_b16 v[40:41], v76 offset:0\n"
    "ds_read_b64_tr_b16 v[42:43], v76 offset:2048\n"
    "ds_read_b64_tr_b16 v[44:45], v77 offset:0\n"
    "ds_read_b64_tr_b16 v[46:47], v77 offset:2048\n"
    "s_waitcnt lgkmcnt(10)\n"
    "buffer_store_dwordx4 v[24:27], v14, s[52:55], s61 offen offset:0 sc1\n"
    "s_waitcnt lgkmcnt(8)\n"
    "buffer_store_dwordx4 v[28:31], v14, s[52:55], s61 offen offset:1024 sc1\n"
    "s_waitcnt lgkmcnt(6)\n"
    "buffer_store_dwordx4 v[32:35], v14, s[52:55], s61 offen offset:2048 sc1\n"
    "v_mfma_f32_16x16x32_bf16 v[80:83], v[24:27], v[96:99], 0\n"
    "v_mfma_f32_16x16x32_bf16 v[80:83], v[28:31], v[100:103], v[80:83]\n"
    "v_mfma_f32_16x16x32_bf16 v[80:83], v[32:35], v[104:107], v[80:83]\n"
    "s_add_u32 s8, s61, 81920\n"
    "s_waitcnt lgkmcnt(4)\n"
    "buffer_store_dwordx4 v[36:39], v14, s[52:55], s8 offen offset:0 sc1\n"
    "s_waitcnt lgkmcnt(2)\n"
    "buffer_store_dwordx4 v[40:43], v14, s[52:55], s8 offen offset:1024 sc1\n"
    "s_waitcnt lgkmcnt(0)\n"
    "buffer_store_dwordx4 v[44:47], v14, s[52:55], s8 offen offset:2048 sc1\n"
    "v_mfma_f32_16x16x32_bf16 v[80:83], v[36:39], v[108:111], v[80:83]\n"
    "v_mfma_f32_16x16x32_bf16 v[80:83], v[40:43], v[112:115], v[80:83]\n"
    "v_mfma_f32_16x16x32_bf16 v[80:83], v[44:47], v[116:119], v[80:83]\n"
    "s_nop 7\n"
    "buffer_store_dwordx4 v[80:83], v78, s[52:55], s26 offen sc1\n"
    "s_waitcnt vmcnt(0) lgkmcnt(0)\n"
    "v_readfirstlane_b32 s64, v12\n"
    "v_readfirstlane_b32 s65, v13\n"
    "s_add_u32 s64, s64, 0x7f4a7c15\n"
    "s_addc_u32 s65, s65, 0x9e3779b9\n"
    "s_barrier\n"
    "v_mov_b32 v16, s64\n"
    "v_mov_b32 v17, s65\n"
    "v_cvt_f32_u32 v18, v0\n"
    "s_add_u32 s98, s4, 98304\n"
    "buffer_store_dwordx2 v[16:17], v18, s[52:55], s98 offen offset:0 sc1\n"
    "buffer_load_dwordx2 v[160:161], v9, s[52:55], 0 offen sc1\n"
    "s_mul_i32 s7, s3, 13\n"
    "ds_read_b64_tr_b16 v[24:25], v6 offset:0\n"
    "ds_read_b64_tr_b16 v[26:27], v6 offset:2048\n"
    "ds_read_b64_tr_b16 v[28:29], v6 offset:4096\n"
    "ds_read_b64_tr_b16 v[30:31], v6 offset:6144\n"
    "ds_read_b64_tr_b16 v[32:33], v6 offset:8192\n"
    "ds_read_b64_tr_b16 v[34:35], v6 offset:10240\n"
    "ds_read_b64_tr_b16 v[36:37], v6 offset:12288\n"
    "ds_read_b64_tr_b16 v[38:39], v6 offset:14336\n"
    "ds_read_b64_tr_b16 v[40:41], v6 offset:16384\n"
    "ds_read_b64_tr_b16 v[42:43], v6 offset:18432\n"
    "s_add_u32 s8, s7, 0\n"
    "s_lshr_b32 s8, s8, 6\n"
    "s_mul_i32 s8, s8, 240640\n"
    "s_add_u32 s8, s8, s63\n"
    "s_waitcnt lgkmcnt(8)\n"
    "buffer_store_dwordx4 v[24:27], v14, s[52:55], s8 offen sc1\n"
    "s_add_u32 s63, s63, 4096\n"
    "s_add_u32 s8, s7, 52\n"
    "s_lshr_b32 s8, s8, 6\n"
    "s_mul_i32 s8, s8, 240640\n"
    "s_add_u32 s8, s8, s63\n"
    "s_waitcnt lgkmcnt(6)\n"
    "buffer_store_dwordx4 v[28:31], v14, s[52:55], s8 offen sc1\n"
    "s_add_u32 s63, s63, 4096\n"
    "s_add_u32 s8, s7, 104\n"
    "s_lshr_b32 s8, s8, 6\n"
    "s_mul_i32 s8, s8, 240640\n"
    "s_add_u32 s8, s8, s63\n"
    "s_waitcnt lgkmcnt(4)\n"
    "buffer_store_dwordx4 v[32:35], v14, s[52:55], s8 offen sc1\n"
    "s_add_u32 s63, s63, 4096\n"
    "s_add_u32 s8, s7, 156\n"
    "s_lshr_b32 s8, s8, 6\n"
    "s_mul_i32 s8, s8, 240640\n"
    "s_add_u32 s8, s8, s63\n"
    "s_waitcnt lgkmcnt(2)\n"
    "buffer_store_dwordx4 v[36:39], v14, s[52:55], s8 offen sc1\n"
    "s_add_u32 s63, s63, 4096\n"
    "s_add_u32 s8, s7, 208\n"
    "s_lshr_b32 s8, s8, 6\n"
    "s_mul_i32 s8, s8, 240640\n"
    "s_add_u32 s8, s8, s63\n"
    "s_waitcnt lgkmcnt(0)\n"
    "buffer_store_dwordx4 v[40:43], v14, s[52:55], s8 offen sc1\n"
    "s_waitcnt vmcnt(5)\n"
    "v_cmp_ne_u64 vcc, s[64:65], v[160:161]\n"
    "s_cmp_eq_u64 vcc, 0\n"
    "s_cbranch_scc1 .L18_%=\n"
    "s_mov_b32 s98, 100000\n"
    ".L20_%=:\n"
    "buffer_load_dwordx2 v[160:161], v9, s[52:55], 0 offen sc1\n"
    "s_waitcnt vmcnt(0)\n"
    "v_cmp_ne_u64 vcc, s[64:65], v[160:161]\n"
    "s_cmp_eq_u64 vcc, 0\n"
    "s_cbranch_scc1 .L18_%=\n"
    "s_sub_u32 s98, s98, 1\n"
    "s_cbranch_scc0 .L20_%=\n"
    ".L18_%=:\n"
    "s_lshl1_add_u32 s44, s16, s45\n"
    "buffer_load_dwordx4 a[0:3], v14, s[52:55], s44 offen offset:0 sc1\n"
    "buffer_load_dwordx4 a[4:7], v14, s[52:55], s44 offen offset:1024 sc1\n"
    "buffer_load_dwordx4 a[8:11], v14, s[52:55], s44 offen offset:2048 sc1\n"
    "buffer_load_dwordx4 a[12:15], v14, s[52:55], s44 offen offset:3072 sc1\n"
    "s_add_u32 s44, s44, 4096\n"
    "buffer_load_dwordx4 a[16:19], v14, s[52:55], s44 offen offset:0 sc1\n"
    "buffer_load_dwordx4 a[20:23], v14, s[52:55], s44 offen offset:1024 sc1\n"
    "s_lshl1_add_u32 s44, s17, s45\n"
    "buffer_load_dwordx4 a[24:27], v14, s[52:55], s44 offen offset:0 sc1\n"
    "buffer_load_dwordx4 a[28:31], v14, s[52:55], s44 offen offset:1024 sc1\n"
    "buffer_load_dwordx4 a[32:35], v14, s[52:55], s44 offen offset:2048 sc1\n"
    "buffer_load_dwordx4 a[36:39], v14, s[52:55], s44 offen offset:3072 sc1\n"
    "s_add_u32 s44, s44, 4096\n"
    "buffer_load_dwordx4 a[40:43], v14, s[52:55], s44 offen offset:0 sc1\n"
    "buffer_load_dwordx4 a[44:47], v14, s[52:55], s44 offen offset:1024 sc1\n"
    "s_lshl1_add_u32 s44, s18, s45\n"
    "buffer_load_dwordx4 a[48:51], v14, s[52:55], s44 offen offset:0 sc1\n"
    "buffer_load_dwordx4 a[52:55], v14, s[52:55], s44 offen offset:1024 sc1\n"
    "buffer_load_dwordx4 a[56:59], v14, s[52:55], s44 offen offset:2048 sc1\n"
    "buffer_load_dwordx4 a[60:63], v14, s[52:55], s44 offen offset:3072 sc1\n"
    "s_add_u32 s44, s44, 4096\n"
    "buffer_load_dwordx4 a[64:67], v14, s[52:55], s44 offen offset:0 sc1\n"
    "buffer_load_dwordx4 a[68:71], v14, s[52:55], s44 offen offset:1024 sc1\n"
    "s_lshl1_add_u32 s44, s19, s45\n"
    "buffer_load_dwordx4 a[72:75], v14, s[52:55], s44 offen offset:0 sc1\n"
    "buffer_load_dwordx4 a[76:79], v14, s[52:55], s44 offen offset:1024 sc1\n"
    "buffer_load_dwordx4 a[80:83], v14, s[52:55], s44 offen offset:2048 sc1\n"
    "buffer_load_dwordx4 a[84:87], v14, s[52:55], s44 offen offset:3072 sc1\n"
    "s_add_u32 s44, s44, 4096\n"
    "buffer_load_dwordx4 a[88:91], v14, s[52:55], s44 offen offset:0 sc1\n"
    "buffer_load_dwordx4 a[92:95], v14, s[52:55], s44 offen offset:1024 sc1\n"
    "buffer_load_dwordx2 v[160:161], v15, s[52:55], s27 offen sc1\n"
    "s_waitcnt vmcnt(0)\n"
    "s_barrier\n"
    "v_mov_b32 v16, s64\n"
    "v_mov_b32 v17, s65\n"
    "v_cvt_f32_u32 v18, v0\n"
    "s_add_u32 s98, s4, 131072\n"
    "buffer_store_dwordx2 v[16:17], v18, s[52:55], s98 offen offset:0 sc1\n"
    "v_cmp_ne_u64 vcc, s[64:65], v[160:161]\n"
    "s_cmp_eq_u64 vcc, 0\n"
    "s_cbranch_scc1 .L21_%=\n"
    "s_mov_b32 s98, 100000\n"
    ".L23_%=:\n"
    "buffer_load_dwordx2 v[160:161], v15, s[52:55], s27 offen sc1\n"
    "s_waitcnt vmcnt(0)\n"
    "v_cmp_ne_u64 vcc, s[64:65], v[160:161]\n"
    "s_cmp_eq_u64 vcc, 0\n"
    "s_cbranch_scc1 .L21_%=\n"
    "s_sub_u32 s98, s98, 1\n"
    "s_cbranch_scc0 .L23_%=\n"
    ".L21_%=:\n"
    "s_lshl1_add_u32 s44, s20, s45\n"
    "buffer_load_dwordx4 a[96:99], v14, s[52:55], s44 offen offset:0 sc1\n"
    "buffer_load_dwordx4 a[100:103], v14, s[52:55], s44 offen offset:1024 sc1\n"
    "buffer_load_dwordx4 a[104:107], v14, s[52:55], s44 offen offset:2048 sc1\n"
    "buffer_load_dwordx4 a[108:111], v14, s[52:55], s44 offen offset:3072 sc1\n"
    "s_add_u32 s44, s44, 4096\n"
    "buffer_load_dwordx4 a[112:115], v14, s[52:55], s44 offen offset:0 sc1\n"
    "buffer_load_dwordx4 a[116:119], v14, s[52:55], s44 offen offset:1024 sc1\n"
    "s_lshl1_add_u32 s44, s21, s45\n"
    "buffer_load_dwordx4 a[120:123], v14, s[52:55], s44 offen offset:0 sc1\n"
    "buffer_load_dwordx4 a[124:127], v14, s[52:55], s44 offen offset:1024 sc1\n"
    "buffer_load_dwordx4 a[128:131], v14, s[52:55], s44 offen offset:2048 sc1\n"
    "buffer_load_dwordx4 a[132:135], v14, s[52:55], s44 offen offset:3072 sc1\n"
    "s_add_u32 s44, s44, 4096\n"
    "buffer_load_dwordx4 a[136:139], v14, s[52:55], s44 offen offset:0 sc1\n"
    "buffer_load_dwordx4 a[140:143], v14, s[52:55], s44 offen offset:1024 sc1\n"
    "s_lshl1_add_u32 s44, s22, s45\n"
    "buffer_load_dwordx4 a[144:147], v14, s[52:55], s44 offen offset:0 sc1\n"
    "buffer_load_dwordx4 a[148:151], v14, s[52:55], s44 offen offset:1024 sc1\n"
    "buffer_load_dwordx4 a[152:155], v14, s[52:55], s44 offen offset:2048 sc1\n"
    "buffer_load_dwordx4 a[156:159], v14, s[52:55], s44 offen offset:3072 sc1\n"
    "s_add_u32 s44, s44, 4096\n"
    "buffer_load_dwordx4 a[160:163], v14, s[52:55], s44 offen offset:0 sc1\n"
    "buffer_load_dwordx4 a[164:167], v14, s[52:55], s44 offen offset:1024 sc1\n"
    "s_lshl1_add_u32 s44, s23, s45\n"
    "buffer_load_dwordx4 a[168:171], v14, s[52:55], s44 offen offset:0 sc1\n"
    "buffer_load_dwordx4 a[172:175], v14, s[52:55], s44 offen offset:1024 sc1\n"
    "buffer_load_dwordx4 a[176:179], v14, s[52:55], s44 offen offset:2048 sc1\n"
    "buffer_load_dwordx4 a[180:183], v14, s[52:55], s44 offen offset:3072 sc1\n"
    "s_add_u32 s44, s44, 4096\n"
    "buffer_load_dwordx4 a[184:187], v14, s[52:55], s44 offen offset:0 sc1\n"
    "buffer_load_dwordx4 a[188:191], v14, s[52:55], s44 offen offset:1024 sc1\n"
    "s_lshl1_add_u32 s44, s24, s45\n"
    "buffer_load_dwordx4 a[192:195], v14, s[52:55], s44 offen offset:0 sc1\n"
    "buffer_load_dwordx4 a[196:199], v14, s[52:55], s44 offen offset:1024 sc1\n"
    "buffer_load_dwordx4 a[200:203], v14, s[52:55], s44 offen offset:2048 sc1\n"
    "buffer_load_dwordx4 a[204:207], v14, s[52:55], s44 offen offset:3072 sc1\n"
    "s_add_u32 s44, s44, 4096\n"
    "buffer_load_dwordx4 a[208:211], v14, s[52:55], s44 offen offset:0 sc1\n"
    "buffer_load_dwordx4 a[212:215], v14, s[52:55], s44 offen offset:1024 sc1\n"
    "buffer_load_dwordx2 v[160:161], v179, s[52:55], 0 offen sc1\n"
    "s_waitcnt vmcnt(0)\n"
    "v_cmp_ne_u64 vcc, s[64:65], v[160:161]\n"
    "s_cmp_eq_u64 vcc, 0\n"
    "s_cbranch_scc1 .L24_%=\n"
    "s_mov_b32 s98, 100000\n"
    ".L26_%=:\n"
    "buffer_load_dwordx2 v[160:161], v179, s[52:55], 0 offen sc1\n"
    "s_waitcnt vmcnt(0)\n"
    "v_cmp_ne_u64 vcc, s[64:65], v[160:161]\n"
    "s_cmp_eq_u64 vcc, 0\n"
    "s_cbranch_scc1 .L24_%=\n"
    "s_sub_u32 s98, s98, 1\n"
    "s_cbranch_scc0 .L26_%=\n"
    ".L24_%=:\n"
    "buffer_load_dwordx4 v[224:227], v162, s[52:55], s16 offen sc1\n"
    "buffer_load_dwordx4 v[192:195], v162, s[52:55], s17 offen sc1\n"
    "buffer_load_dwordx4 v[128:131], v162, s[52:55], s18 offen sc1\n"
    "buffer_load_dwordx4 v[228:231], v162, s[52:55], s19 offen sc1\n"
    "buffer_load_dwordx4 v[196:199], v162, s[52:55], s20 offen sc1\n"
    "buffer_load_dwordx4 v[132:135], v162, s[52:55], s21 offen sc1\n"
    "buffer_load_dwordx4 v[232:235], v162, s[52:55], s22 offen sc1\n"
    "buffer_load_dwordx4 v[200:203], v162, s[52:55], s23 offen sc1\n"
    "buffer_load_dwordx4 v[136:139], v162, s[52:55], s24 offen sc1\n"
    "buffer_load_dwordx4 v[236:239], v162, s[52:55], s25 offen sc1\n"
    "v_add_u32 v162, 40960, v162\n"
    "s_waitcnt vmcnt(9)\n"
    "ds_write2st64_b64 v164, v[224:225], v[226:227] offset0:80 offset1:82\n"
    "s_waitcnt vmcnt(8)\n"
    "ds_write2st64_b64 v164, v[192:193], v[194:195] offset0:88 offset1:90\n"
    "s_waitcnt vmcnt(7)\n"
    "ds_write2st64_b64 v164, v[128:129], v[130:131] offset0:96 offset1:98\n"
    "s_waitcnt vmcnt(6)\n"
    "ds_write2st64_b64 v164, v[228:229], v[230:231] offset0:104 offset1:106\n"
    "s_waitcnt vmcnt(5)\n"
    "ds_write2st64_b64 v164, v[196:197], v[198:199] offset0:112 offset1:114\n"
    "s_waitcnt vmcnt(4)\n"
    "ds_write2st64_b64 v164, v[132:133], v[134:135] offset0:120 offset1:122\n"
    "s_waitcnt vmcnt(3)\n"
    "ds_write2st64_b64 v164, v[232:233], v[234:235] offset0:128 offset1:130\n"
    "s_waitcnt vmcnt(2)\n"
    "ds_write2st64_b64 v164, v[200:201], v[202:203] offset0:136 offset1:138\n"
    "s_waitcnt vmcnt(1)\n"
    "ds_write2st64_b64 v164, v[136:137], v[138:139] offset0:144 offset1:146\n"
    "s_waitcnt vmcnt(0)\n"
    "ds_write2st64_b64 v164, v[236:237], v[238:239] offset0:152 offset1:154\n"
    ".J1_%=:\n"
    "s_waitcnt lgkmcnt(0)\n"
    "s_barrier\n"
    "ds_read_b128 v[120:123], v166 offset:0\n"
    "ds_read_b128 v[124:127], v168 offset:0\n"
    "ds_read_b128 v[128:131], v167 offset:2048\n"
    "ds_read_b128 v[132:135], v169 offset:2048\n"
    "buffer_load_dwordx2 v[180:181], v179, s[52:55], s27 offen sc1\n"
    "s_waitcnt lgkmcnt(3)\n"
    "v_mfma_f32_16x16x32_bf16 v[16:19], v[120:123], v[120:123], 0\n"
    "ds_read_b128 v[136:139], v166 offset:4096\n"
    "v_and_b32 v119, 15, v1\n"
    "v_mfma_f32_16x16x32_bf16 v[20:23], v[144:147], v[120:123], 0\n"
    "ds_read_b128 v[140:143], v168 offset:4096\n"
    "v_min_u32 v119, 11, v119\n"
    "v_mfma_f32_16x16x32_bf16 v[64:67], a[0:3], v[120:123], 0\n"
    "v_and_b32 v118, 16, v1\n"
    "v_add_lshl_u32 v119, v119, v118, 4\n"
    "v_mfma_f32_16x16x32_bf16 v[68:71], a[4:7], v[120:123], 0\n"
    "v_lshrrev_b32 v118, 5, v1\n"
    "v_lshl_add_u32 v119, v118, 13, v119\n"
    "v_mfma_f32_16x16x32_bf16 v[72:75], a[8:11], v[120:123], 0\n"
    "s_waitcnt lgkmcnt(4)\n"
    "s_add_u32 s44, s45, 6144\n"
    "v_mfma_f32_16x16x32_bf16 v[76:79], a[0:3], v[124:127], 0\n"
    "buffer_load_dwordx4 v[196:199], v119, s[52:55], s44 offen sc1\n"
    "v_mfma_f32_16x16x32_bf16 v[80:83], a[4:7], v[124:127], 0\n"
    "s_add_u32 s44, s45, 22528\n"
    "v_mfma_f32_16x16x32_bf16 v[84:87], a[8:11], v[124:127], 0\n"
    "s_waitcnt lgkmcnt(3)\n"
    "v_mfma_f32_16x16x32_bf16 v[64:67], a[12:15], v[128:131], v[64:67]\n"
    "ds_read_b128 v[120:123], v167 offset:6144\n"
    "v_mfma_f32_16x16x32_bf16 v[68:71], a[16:19], v[128:131], v[68:71]\n"
    "ds_read_b128 v[124:127], v169 offset:6144\n"
    "v_mfma_f32_16x16x32_bf16 v[72:75], a[20:23], v[128:131], v[72:75]\n"
    "s_waitcnt lgkmcnt(4)\n"
    "buffer_load_dwordx4 v[200:203], v119, s[52:55], s44 offen sc1\n"
    "v_mfma_f32_16x16x32_bf16 v[76:79], a[12:15], v[132:135], v[76:79]\n"
    "s_add_u32 s44, s45, 38912\n"
    "v_mfma_f32_16x16x32_bf16 v[80:83], a[16:19], v[132:135], v[80:83]\n"
    "v_mfma_f32_16x16x32_bf16 v[84:87], a[20:23], v[132:135], v[84:87]\n"
    "s_waitcnt lgkmcnt(3)\n"
    "buffer_load_dwordx4 v[204:207], v119, s[52:55], s44 offen sc1\n"
    "v_mfma_f32_16x16x32_bf16 v[16:19], v[136:139], v[136:139], v[16:19]\n"
    "ds_read_b128 v[128:131], v166 offset:8192\n"
    "s_add_u32 s44, s45, 55296\n"
    "v_mfma_f32_16x16x32_bf16 v[20:23], v[144:147], v[136:139], v[20:23]\n"
    "ds_read_b128 v[132:135], v168 offset:8192\n"
    "v_mfma_f32_16x16x32_bf16 v[64:67], a[24:27], v[136:139], v[64:67]\n"
    "buffer_load_dwordx4 v[208:211], v119, s[52:55], s44 offen sc1\n"
    "v_mfma_f32_16x16x32_bf16 v[68:71], a[28:31], v[136:139], v[68:71]\n"
    "s_add_u32 s44, s45, 71680\n"
    "v_mfma_f32_16x16x32_bf16 v[72:75], a[32:35], v[136:139], v[72:75]\n"
    "s_waitcnt lgkmcnt(4)\n"
    "v_mfma_f32_16x16x32_bf16 v[76:79], a[24:27], v[140:143], v[76:79]\n"
    "buffer_load_dwordx4 v[212:215], v119, s[52:55], s44 offen sc1\n"
    "v_mfma_f32_16x16x32_bf16 v[80:83], a[28:31], v[140:143], v[80:83]\n"
    "v_mfma_f32_16x16x32_bf16 v[84:87], a[32:35], v[140:143], v[84:87]\n"
    "s_waitcnt lgkmcnt(3)\n"
    "v_mfma_f32_16x16x32_bf16 v[64:67], a[36:39], v[120:123], v[64:67]\n"
    "ds_read_b128 v[136:139], v167 offset:10240\n"
    "v_mfma_f32_16x16x32_bf16 v[68:71], a[40:43], v[120:123], v[68:71]\n"
    "ds_read_b128 v[140:143], v169 offset:10240\n"
    "v_mfma_f32_16x16x32_bf16 v[72:75], a[44:47], v[120:123], v[72:75]\n"
    "s_waitcnt lgkmcnt(4)\n"
    "v_mfma_f32_16x16x32_bf16 v[76:79], a[36:39], v[124:127], v[76:79]\n"
    "v_mfma_f32_16x16x32_bf16 v[80:83], a[40:43], v[124:127], v[80:83]\n"
    "v_mfma_f32_16x16x32_bf16 v[84:87], a[44:47], v[124:127], v[84:87]\n"
    "s_waitcnt lgkmcnt(3)\n"
    "v_mfma_f32_16x16x32_bf16 v[16:19], v[128:131], v[128:131], v[16:19]\n"
    "ds_read_b128 v[120:123], v166 offset:12288\n"
    "s_waitcnt vmcnt(0)\n"
    "s_barrier\n"
    "v_mov_b32 v116, s64\n"
    "v_mov_b32 v117, s65\n"
    "v_cvt_f32_u32 v118, v0\n"
    "s_add_u32 s98, s4, 262144\n"
    "buffer_store_dwordx2 v[116:117], v118, s[52:55], s98 offen offset:0 sc1\n"
    "v_cmp_ne_u64 vcc, s[64:65], v[180:181]\n"
    "s_cmp_eq_u64 vcc, 0\n"
    "s_cbranch_scc1 .L27_%=\n"
    "s_mov_b32 s98, 100000\n"
    ".L29_%=:\n"
    "buffer_load_dwordx2 v[180:181], v179, s[52:55], s27 offen sc1\n"
    "s_waitcnt vmcnt(0)\n"
    "v_cmp_ne_u64 vcc, s[64:65], v[180:181]\n"
    "s_cmp_eq_u64 vcc, 0\n"
    "s_cbranch_scc1 .L27_%=\n"
    "s_sub_u32 s98, s98, 1\n"
    "s_cbranch_scc0 .L29_%=\n"
    ".L27_%=:\n"
    "v_mfma_f32_16x16x32_bf16 v[20:23], v[144:147], v[128:131], v[20:23]\n"
    "ds_read_b128 v[124:127], v168 offset:12288\n"
    "v_add_f32 v196, v196, v200\n"
    "v_mfma_f32_16x16x32_bf16 v[64:67], a[48:51], v[128:131], v[64:67]\n"
    "buffer_load_dwordx4 v[24:27], v162, s[52:55], s16 offen sc1\n"
    "v_mfma_f32_16x16x32_bf16 v[68:71], a[52:55], v[128:131], v[68:71]\n"
    "v_add_f32 v197, v197, v201\n"
    "v_add_f32 v198, v198, v202\n"
    "v_mfma_f32_16x16x32_bf16 v[72:75], a[56:59], v[128:131], v[72:75]\n"
    "s_waitcnt lgkmcnt(4)\n"
    "v_add_f32 v199, v199, v203\n"
    "v_add_f32 v196, v196, v204\n"
    "v_mfma_f32_16x16x32_bf16 v[76:79], a[48:51], v[132:135], v[76:79]\n"
    "buffer_load_dwordx4 v[28:31], v162, s[52:55], s17 offen sc1\n"
    "v_mfma_f32_16x16x32_bf16 v[80:83], a[52:55], v[132:135], v[80:83]\n"
    "v_add_f32 v197, v197, v205\n"
    "v_add_f32 v198, v198, v206\n"
    "v_mfma_f32_16x16x32_bf16 v[84:87], a[56:59], v[132:135], v[84:87]\n"
    "s_waitcnt lgkmcnt(3)\n"
    "v_add_f32 v199, v199, v207\n"
    "v_add_f32 v196, v196, v208\n"
    "v_mfma_f32_16x16x32_bf16 v[64:67], a[60:63], v[136:139], v[64:67]\n"
    "ds_read_b128 v[128:131], v167 offset:14336\n"
    "v_add_f32 v197, v197, v209\n"
    "v_mfma_f32_16x16x32_bf16 v[68:71], a[64:67], v[136:139], v[68:71]\n"
    "ds_read_b128 v[132:135], v169 offset:14336\n"
    "v_add_f32 v198, v198, v210\n"
    "v_mfma_f32_16x16x32_bf16 v[72:75], a[68:71], v[136:139], v[72:75]\n"
    "s_waitcnt lgkmcnt(4)\n"
    "buffer_load_dwordx4 v[32:35], v162, s[52:55], s18 offen sc1\n"
    "v_mfma_f32_16x16x32_bf16 v[76:79], a[60:63], v[140:143], v[76:79]\n"
    "v_add_f32 v199, v199, v211\n"
    "v_add_f32 v196, v196, v212\n"
    "v_mfma_f32_16x16x32_bf16 v[80:83], a[64:67], v[140:143], v[80:83]\n"
    "v_add_f32 v197, v197, v213\n"
    "v_add_f32 v198, v198, v214\n"
    "v_mfma_f32_16x16x32_bf16 v[84:87], a[68:71], v[140:143], v[84:87]\n"
    "s_waitcnt lgkmcnt(3)\n"
    "buffer_load_dwordx4 v[36:39], v162, s[52:55], s19 offen sc1\n"
    "v_mfma_f32_16x16x32_bf16 v[16:19], v[120:123], v[120:123], v[16:19]\n"
    "ds_read_b128 v[136:139], v166 offset:16384\n"
    "v_add_f32 v199, v199, v215\n"
    "v_mfma_f32_16x16x32_bf16 v[20:23], v[144:147], v[120:123], v[20:23]\n"
    "ds_read_b128 v[140:143], v168 offset:16384\n"
    "v_mfma_f32_16x16x32_bf16 v[64:67], a[72:75], v[120:123], v[64:67]\n"
    "buffer_load_dwordx4 v[40:43], v162, s[52:55], s20 offen sc1\n"
    "v_mfma_f32_16x16x32_bf16 v[68:71], a[76:79], v[120:123], v[68:71]\n"
    "s_lshl1_add_u32 s44, s25, s45\n"
    "v_permlane32_swap_b32 v196, v197\n"
    "v_mfma_f32_16x16x32_bf16 v[72:75], a[80:83], v[120:123], v[72:75]\n"
    "s_waitcnt lgkmcnt(4)\n"
    "v_permlane32_swap_b32 v198, v199\n"
    "v_mfma_f32_16x16x32_bf16 v[76:79], a[72:75], v[124:127], v[76:79]\n"
    "buffer_load_dwordx4 a[216:219], v165, s[52:55], s44 offen offset:0 sc1\n"
    "v_mfma_f32_16x16x32_bf16 v[80:83], a[76:79], v[124:127], v[80:83]\n"
    "s_lshl_b32 s9, s3, 8\n"
    "s_add_u32 s9, s9, 62464\n"
    "v_and_b32 v200, 15, v1\n"
    "v_mfma_f32_16x16x32_bf16 v[84:87], a[80:83], v[124:127], v[84:87]\n"
    "s_waitcnt lgkmcnt(3)\n"
    "v_lshl_add_u32 v200, v200, 4, s9\n"
    "v_add_f32 v196, v196, v197\n"
    "v_mfma_f32_16x16x32_bf16 v[64:67], a[84:87], v[128:131], v[64:67]\n"
    "ds_read_b128 v[120:123], v167 offset:18432\n"
    "v_add_f32 v198, v198, v199\n"
    "v_mfma_f32_16x16x32_bf16 v[68:71], a[88:91], v[128:131], v[68:71]\n"
    "ds_read_b128 v[124:127], v169 offset:18432\n"
    "v_and_b32 v118, 16, v1\n"
    "v_mfma_f32_16x16x32_bf16 v[72:75], a[92:95], v[128:131], v[72:75]\n"
    "s_waitcnt lgkmcnt(4)\n"
    "buffer_load_dwordx4 a[220:223], v165, s[52:55], s44 offen offset:1024 sc1\n"
    "v_mfma_f32_16x16x32_bf16 v[76:79], a[84:87], v[132:135], v[76:79]\n"
    "v_lshrrev_b32 v201, 5, v1\n"
    "v_lshl_add_u32 v200, v201, 2, v200\n"
    "v_mfma_f32_16x16x32_bf16 v[80:83], a[88:91], v[132:135], v[80:83]\n"
    "v_permlane16_swap_b32 v196, v198\n"
    "v_mfma_f32_16x16x32_bf16 v[84:87], a[92:95], v[132:135], v[84:87]\n"
    "s_waitcnt lgkmcnt(3)\n"
    "buffer_load_dwordx4 a[224:227], v165, s[52:55], s44 offen offset:2048 sc1\n"
    "v_mfma_f32_16x16x32_bf16 v[16:19], v[136:139], v[136:139], v[16:19]\n"
    "ds_read_b128 v[128:131], v166 offset:20480\n"
    "v_lshrrev_b32 v201, 1, v118\n"
    "v_mfma_f32_16x16x32_bf16 v[20:23], v[144:147], v[136:139], v[20:23]\n"
    "ds_read_b128 v[132:135], v168 offset:20480\n"
    "v_add_u32 v200, v200, v201\n"
    "v_mfma_f32_16x16x32_bf16 v[64:67], a[96:99], v[136:139], v[64:67]\n"
    "buffer_load_dwordx2 v[160:161], v163, s[52:55], s27 offen sc1\n"
    "v_mfma_f32_16x16x32_bf16 v[68:71], a[100:103], v[136:139], v[68:71]\n"
    "v_add_f32 v196, v196, v198\n"
    "v_add_u32 v118, s9, v118\n"
    "v_mfma_f32_16x16x32_bf16 v[72:75], a[104:107], v[136:139], v[72:75]\n"
    "s_waitcnt lgkmcnt(4)\n"
    "ds_write_b32 v200, v196\n"
    "v_mfma_f32_16x16x32_bf16 v[76:79], a[96:99], v[140:143], v[76:79]\n"
    "buffer_load_dwordx4 a[228:231], v165, s[52:55], s44 offen offset:3072 sc1\n"
    "v_mfma_f32_16x16x32_bf16 v[80:83], a[100:103], v[140:143], v[80:83]\n"
    "s_add_u32 s44, s44, 4096\n"
    "v_mfma_f32_16x16x32_bf16 v[84:87], a[104:107], v[140:143], v[84:87]\n"
    "s_waitcnt lgkmcnt(4)\n"
    "ds_read_b128 v[148:151], v118 offset:0\n"
    "v_mfma_f32_16x16x32_bf16 v[64:67], a[108:111], v[120:123], v[64:67]\n"
    "ds_read_b128 v[136:139], v167 offset:22528\n"
    "v_mfma_f32_16x16x32_bf16 v[68:71], a[112:115], v[120:123], v[68:71]\n"
    "ds_read_b128 v[140:143], v169 offset:22528\n"
    "v_mfma_f32_16x16x32_bf16 v[72:75], a[116:119], v[120:123], v[72:75]\n"
    "s_waitcnt lgkmcnt(6)\n"
    "buffer_load_dwordx4 a[232:235], v165, s[52:55], s44 offen offset:0 sc1\n"
    "v_mfma_f32_16x16x32_bf16 v[76:79], a[108:111], v[124:127], v[76:79]\n"
    "ds_read_b128 v[184:187], v118 offset:96\n"
    "v_mfma_f32_16x16x32_bf16 v[80:83], a[112:115], v[124:127], v[80:83]\n"
    "ds_read_b128 v[152:155], v118 offset:64\n"
    "v_mfma_f32_16x16x32_bf16 v[84:87], a[116:119], v[124:127], v[84:87]\n"
    "s_waitcnt lgkmcnt(7)\n"
    "buffer_load_dwordx4 a[236:239], v165, s[52:55], s44 offen offset:1024 sc1\n"
    "v_mfma_f32_16x16x32_bf16 v[16:19], v[128:131], v[128:131], v[16:19]\n"
    "ds_read_b128 v[120:123], v166 offset:24576\n"
    "v_mfma_f32_16x16x32_bf16 v[20:23], v[144:147], v[128:131], v[20:23]\n"
    "ds_read_b128 v[124:127], v168 offset:24576\n"
    "v_mfma_f32_16x16x32_bf16 v[64:67], a[120:123], v[128:131], v[64:67]\n"
    "buffer_load_dwordx4 v[44:47], v162, s[52:55], s21 offen sc1\n"
    "v_mfma_f32_16x16x32_bf16 v[68:71], a[124:127], v[128:131], v[68:71]\n"
    "s_waitcnt vmcnt(12)\n"
    "ds_write2st64_b64 v164, v[24:25], v[26:27] offset0:0 offset1:2\n"
    "v_mfma_f32_16x16x32_bf16 v[72:75], a[128:131], v[128:131], v[72:75]\n"
    "s_waitcnt lgkmcnt(9)\n"
    "v_mfma_f32_16x16x32_bf16 v[76:79], a[120:123], v[132:135], v[76:79]\n"
    "v_mfma_f32_16x16x32_bf16 v[80:83], a[124:127], v[132:135], v[80:83]\n"
    "buffer_load_dwordx4 v[48:51], v162, s[52:55], s22 offen sc1\n"
    "v_mfma_f32_16x16x32_bf16 v[84:87], a[128:131], v[132:135], v[84:87]\n"
    "s_waitcnt lgkmcnt(6)\n"
    "ds_read_b128 v[188:191], v118 offset:32\n"
    "v_mfma_f32_16x16x32_bf16 v[64:67], a[132:135], v[136:139], v[64:67]\n"
    "ds_read_b128 v[128:131], v167 offset:26624\n"
    "v_mfma_f32_16x16x32_bf16 v[68:71], a[136:139], v[136:139], v[68:71]\n"
    "ds_read_b128 v[132:135], v169 offset:26624\n"
    "v_mfma_f32_16x16x32_bf16 v[72:75], a[140:143], v[136:139], v[72:75]\n"
    "s_waitcnt lgkmcnt(8)\n"
    "buffer_load_dwordx4 v[52:55], v162, s[52:55], s23 offen sc1\n"
    "v_mfma_f32_16x16x32_bf16 v[76:79], a[132:135], v[140:143], v[76:79]\n"
    "s_waitcnt vmcnt(13)\n"
    "ds_write2st64_b64 v164, v[28:29], v[30:31] offset0:8 offset1:10\n"
    "v_mfma_f32_16x16x32_bf16 v[80:83], a[136:139], v[140:143], v[80:83]\n"
    "v_mfma_f32_16x16x32_bf16 v[84:87], a[140:143], v[140:143], v[84:87]\n"
    "s_waitcnt lgkmcnt(6)\n"
    "v_mfma_f32_16x16x32_bf16 v[16:19], v[120:123], v[120:123], v[16:19]\n"
    "ds_read_b128 v[136:139], v166 offset:28672\n"
    "v_mfma_f32_16x16x32_bf16 v[20:23], v[144:147], v[120:123], v[20:23]\n"
    "ds_read_b128 v[140:143], v168 offset:28672\n"
    "v_mfma_f32_16x16x32_bf16 v[64:67], a[144:147], v[120:123], v[64:67]\n"
    "buffer_load_dwordx4 v[56:59], v162, s[52:55], s24 offen sc1\n"
    "v_mfma_f32_16x16x32_bf16 v[68:71], a[148:151], v[120:123], v[68:71]\n"
    "s_waitcnt vmcnt(13)\n"
    "ds_write2st64_b64 v164, v[32:33], v[34:35] offset0:16 offset1:18\n"
    "v_mfma_f32_16x16x32_bf16 v[72:75], a[152:155], v[120:123], v[72:75]\n"
    "s_waitcnt lgkmcnt(8)\n"
    "v_mfma_f32_16x16x32_bf16 v[76:79], a[144:147], v[124:127], v[76:79]\n"
    "buffer_load_dwordx4 v[60:63], v162, s[52:55], s25 offen sc1\n"
    "v_mfma_f32_16x16x32_bf16 v[80:83], a[148:151], v[124:127], v[80:83]\n"
    "ds_read_b128 v[156:159], v118 offset:128\n"
    "v_mfma_f32_16x16x32_bf16 v[84:87], a[152:155], v[124:127], v[84:87]\n"
    "s_waitcnt lgkmcnt(6)\n"
    "ds_read_b128 v[192:195], v118 offset:160\n"
    "v_mfma_f32_16x16x32_bf16 v[64:67], a[156:159], v[128:131], v[64:67]\n"
    "ds_read_b128 v[120:123], v167 offset:30720\n"
    "s_waitcnt vmcnt(8)\n"
    "v_cmp_ne_u64 vcc, s[64:65], v[160:161]\n"
    "s_cmp_eq_u64 vcc, 0\n"
    "s_cbranch_scc1 .L30_%=\n"
    "s_mov_b32 s98, 100000\n"
    ".L32_%=:\n"
    "buffer_load_dwordx2 v[160:161], v163, s[52:55], s27 offen sc1\n"
    "s_waitcnt vmcnt(0)\n"
    "v_cmp_ne_u64 vcc, s[64:65], v[160:161]\n"
    "s_cmp_eq_u64 vcc, 0\n"
    "s_cbranch_scc1 .L30_%=\n"
    "s_sub_u32 s98, s98, 1\n"
    "s_cbranch_scc0 .L32_%=\n"
    ".L30_%=:\n"
    "v_mfma_f32_16x16x32_bf16 v[68:71], a[160:163], v[128:131], v[68:71]\n"
    "ds_read_b128 v[124:127], v169 offset:30720\n"
    "s_add_u32 s43, s39, 46080\n"
    "v_mfma_f32_16x16x32_bf16 v[72:75], a[164:167], v[128:131], v[72:75]\n"
    "s_waitcnt lgkmcnt(8)\n"
    "ds_write2st64_b64 v164, v[36:37], v[38:39] offset0:24 offset1:26\n"
    "v_mfma_f32_16x16x32_bf16 v[76:79], a[156:159], v[132:135], v[76:79]\n"
    "buffer_load_dwordx4 v[196:199], v165, s[52:55], s43 offen offset:0 sc1\n"
    "v_mfma_f32_16x16x32_bf16 v[80:83], a[160:163], v[132:135], v[80:83]\n"
    "s_cmp_eq_u32 s2, 0\n"
    "s_cselect_b32 s9, 63, 39\n"
    "v_min_u32 v163, s9, v1\n"
    "v_mfma_f32_16x16x32_bf16 v[84:87], a[164:167], v[132:135], v[84:87]\n"
    "s_waitcnt lgkmcnt(7)\n"
    "s_lshl_b32 s9, s3, 9\n"
    "s_add_u32 s9, s9, 36864\n"
    "v_and_b32 v160, 3, v163\n"
    "v_mfma_f32_16x16x32_bf16 v[16:19], v[136:139], v[136:139], v[16:19]\n"
    "ds_read_b128 v[128:131], v166 offset:32768\n"
    "v_lshrrev_b32 v163, 2, v163\n"
    "v_mfma_f32_16x16x32_bf16 v[20:23], v[144:147], v[136:139], v[20:23]\n"
    "ds_read_b128 v[132:135], v168 offset:32768\n"
    "v_lshl_add_u32 v160, v160, 7, s9\n"
    "v_mfma_f32_16x16x32_bf16 v[64:67], a[168:171], v[136:139], v[64:67]\n"
    "ds_write2st64_b64 v164, v[40:41], v[42:43] offset0:32 offset1:34\n"
    "v_mfma_f32_16x16x32_bf16 v[68:71], a[172:175], v[136:139], v[68:71]\n"
    "buffer_load_dwordx4 v[200:203], v165, s[52:55], s43 offen offset:1024 sc1\n"
    "v_mfma_f32_16x16x32_bf16 v[72:75], a[176:179], v[136:139], v[72:75]\n"
    "s_waitcnt lgkmcnt(9)\n"
    "v_lshl_add_u32 v163, v163, 11, v160\n"
    "v_add_u32 v162, 40960, v162\n"
    "v_mfma_f32_16x16x32_bf16 v[76:79], a[168:171], v[140:143], v[76:79]\n"
    "v_mfma_f32_16x16x32_bf16 v[80:83], a[172:175], v[140:143], v[80:83]\n"
    "buffer_load_dwordx4 v[204:207], v165, s[52:55], s43 offen offset:2048 sc1\n"
    "v_mfma_f32_16x16x32_bf16 v[84:87], a[176:179], v[140:143], v[84:87]\n"
    "s_waitcnt lgkmcnt(5)\n"
    "v_mfma_f32_16x16x32_bf16 v[64:67], a[180:183], v[120:123], v[64:67]\n"
    "ds_read_b128 v[136:139], v167 offset:34816\n"
    "v_mfma_f32_16x16x32_bf16 v[68:71], a[184:187], v[120:123], v[68:71]\n"
    "ds_read_b128 v[140:143], v169 offset:34816\n"
    "v_mfma_f32_16x16x32_bf16 v[72:75], a[188:191], v[120:123], v[72:75]\n"
    "s_waitcnt lgkmcnt(6)\n"
    "buffer_load_dwordx4 v[208:211], v165, s[52:55], s43 offen offset:3072 sc1\n"
    "v_mfma_f32_16x16x32_bf16 v[76:79], a[180:183], v[124:127], v[76:79]\n"
    "v_mfma_f32_16x16x32_bf16 v[80:83], a[184:187], v[124:127], v[80:83]\n"
    "v_mfma_f32_16x16x32_bf16 v[84:87], a[188:191], v[124:127], v[84:87]\n"
    "s_waitcnt lgkmcnt(4)\n"
    "v_mfma_f32_16x16x32_bf16 v[16:19], v[128:131], v[128:131], v[16:19]\n"
    "ds_read_b128 v[120:123], v166 offset:36864\n"
    "v_mfma_f32_16x16x32_bf16 v[20:23], v[144:147], v[128:131], v[20:23]\n"
    "ds_read_b128 v[124:127], v168 offset:36864\n"
    "v_mfma_f32_16x16x32_bf16 v[64:67], a[192:195], v[128:131], v[64:67]\n"
    "s_waitcnt vmcnt(8)\n"
    "ds_write2st64_b64 v164, v[44:45], v[46:47] offset0:40 offset1:42\n"
    "v_mfma_f32_16x16x32_bf16 v[68:71], a[196:199], v[128:131], v[68:71]\n"
    "buffer_load_dwordx2 v[160:161], v163, s[52:55], s29 offen sc1\n"
    "v_mfma_f32_16x16x32_bf16 v[72:75], a[200:203], v[128:131], v[72:75]\n"
    "s_waitcnt lgkmcnt(6)\n"
    "s_add_u32 s43, s39, 50176\n"
    "v_mfma_f32_16x16x32_bf16 v[76:79], a[192:195], v[132:135], v[76:79]\n"
    "s_waitcnt vmcnt(8)\n"
    "ds_write2st64_b64 v164, v[48:49], v[50:51] offset0:48 offset1:50\n"
    "v_mfma_f32_16x16x32_bf16 v[80:83], a[196:199], v[132:135], v[80:83]\n"
    "buffer_load_dwordx4 v[212:215], v165, s[52:55], s43 offen offset:0 sc1\n"
    "v_mfma_f32_16x16x32_bf16 v[84:87], a[200:203], v[132:135], v[84:87]\n"
    "s_waitcnt lgkmcnt(5)\n"
    "v_mfma_f32_16x16x32_bf16 v[64:67], a[204:207], v[136:139], v[64:67]\n"
    "ds_read_b128 v[128:131], v167 offset:38912\n"
    "v_mfma_f32_16x16x32_bf16 v[68:71], a[208:211], v[136:139], v[68:71]\n"
    "ds_read_b128 v[132:135], v169 offset:38912\n"
    "v_mfma_f32_16x16x32_bf16 v[72:75], a[212:215], v[136:139], v[72:75]\n"
    "s_waitcnt lgkmcnt(6)\n"
    "s_waitcnt vmcnt(8)\n"
    "ds_write2st64_b64 v164, v[52:53], v[54:55] offset0:56 offset1:58\n"
    "v_mfma_f32_16x16x32_bf16 v[76:79], a[204:207], v[140:143], v[76:79]\n"
    "buffer_load_dwordx4 v[216:219], v165, s[52:55], s43 offen offset:1024 sc1\n"
    "v_mfma_f32_16x16x32_bf16 v[80:83], a[208:211], v[140:143], v[80:83]\n"
    "v_mfma_f32_16x16x32_bf16 v[84:87], a[212:215], v[140:143], v[84:87]\n"
    "s_waitcnt lgkmcnt(6)\n"
    "v_mfma_f32_16x16x32_bf16 v[16:19], v[120:123], v[120:123], v[16:19]\n"
    "s_waitcnt vmcnt(8)\n"
    "ds_write2st64_b64 v164, v[56:57], v[58:59] offset0:64 offset1:66\n"
    "v_mfma_f32_16x16x32_bf16 v[20:23], v[144:147], v[120:123], v[20:23]\n"
    "buffer_load_dwordx4 v[220:223], v165, s[52:55], s43 offen offset:2048 sc1\n"
    "v_mfma_f32_16x16x32_bf16 v[64:67], a[216:219], v[120:123], v[64:67]\n"
    "v_mfma_f32_16x16x32_bf16 v[68:71], a[220:223], v[120:123], v[68:71]\n"
    "s_waitcnt vmcnt(8)\n"
    "ds_write2st64_b64 v164, v[60:61], v[62:63] offset0:72 offset1:74\n"
    "v_mfma_f32_16x16x32_bf16 v[72:75], a[224:227], v[120:123], v[72:75]\n"
    "s_waitcnt lgkmcnt(7)\n"
    "v_mfma_f32_16x16x32_bf16 v[76:79], a[216:219], v[124:127], v[76:79]\n"
    "v_cndmask_b32 v21, v16, v17, s[32:33]\n"
    "v_cndmask_b32 v21, v21, v18, s[34:35]\n"
    "v_mfma_f32_16x16x32_bf16 v[80:83], a[220:223], v[124:127], v[80:83]\n"
    "v_cndmask_b32 v21, v21, v19, s[36:37]\n"
    "ds_write_b64 v174, v[20:21] offset:0\n"
    "v_mfma_f32_16x16x32_bf16 v[84:87], a[224:227], v[124:127], v[84:87]\n"
    "s_waitcnt lgkmcnt(5)\n"
    "s_waitcnt lgkmcnt(0)\n"
    "s_barrier\n"
    "ds_read_b128 v[136:139], v170 offset:0\n"
    "ds_read_b128 v[140:143], v172 offset:0\n"
    "v_mfma_f32_16x16x32_bf16 v[64:67], a[228:231], v[128:131], v[64:67]\n"
    "ds_read_b128 v[120:123], v171 offset:2048\n"
    "v_mfma_f32_16x16x32_bf16 v[68:71], a[232:235], v[128:131], v[68:71]\n"
    "ds_read_b128 v[124:127], v173 offset:2048\n"
    "v_mfma_f32_16x16x32_bf16 v[72:75], a[236:239], v[128:131], v[72:75]\n"
    "ds_read_b128 v[8:11], v175 offset:0\n"
    "v_mfma_f32_16x16x32_bf16 v[76:79], a[228:231], v[132:135], v[76:79]\n"
    "s_waitcnt vmcnt(7)\n"
    "ds_write_b128 v178, v[196:199] offset:0\n"
    "v_mfma_f32_16x16x32_bf16 v[80:83], a[232:235], v[132:135], v[80:83]\n"
    "v_mfma_f32_16x16x32_bf16 v[84:87], a[236:239], v[132:135], v[84:87]\n"
    "s_waitcnt lgkmcnt(5)\n"
    "v_mfma_f32_16x16x32_bf16 v[16:19], v[136:139], v[136:139], 0\n"
    "ds_read_b128 v[128:131], v170 offset:4096\n"
    "v_mfma_f32_16x16x32_bf16 v[20:23], v[144:147], v[136:139], 0\n"
    "ds_read_b128 v[132:135], v172 offset:4096\n"
    "v_mfma_f32_16x16x32_bf16 v[88:91], a[0:3], v[136:139], 0\n"
    "s_waitcnt vmcnt(6)\n"
    "ds_write_b128 v178, v[200:203] offset:4096\n"
    "v_mfma_f32_16x16x32_bf16 v[92:95], a[4:7], v[136:139], 0\n"
    "s_waitcnt lgkmcnt(4)\n"
    "v_add_f32 v8, v8, v10\n"
    "v_add_f32 v9, v9, v11\n"
    "v_mul_f32 v8, 0x3acccccd, v8\n"
    "v_mfma_f32_16x16x32_bf16 v[96:99], a[8:11], v[136:139], 0\n"
    "v_mul_f32 v9, 0x3acccccd, v9\n"
    "v_fma_f32 v9, -v8, v8, v9\n"
    "v_max_f32 v9, 0, v9\n"
    "v_mfma_f32_16x16x32_bf16 v[100:103], a[0:3], v[140:143], 0\n"
    "s_waitcnt vmcnt(5)\n"
    "ds_write_b128 v178, v[204:207] offset:8192\n"
    "v_mfma_f32_16x16x32_bf16 v[104:107], a[4:7], v[140:143], 0\n"
    "v_cvt_pk_bf16_f32 v8, -v8, v9\n"
    "v_and_b32 v9, 0xffff0000, v8\n"
    "v_lshlrev_b32 v12, 16, v8\n"
    "v_add_f32 v9, 0x3727c5ac, v9\n"
    "v_mfma_f32_16x16x32_bf16 v[108:111], a[8:11], v[140:143], 0\n"
    "s_waitcnt vmcnt(3)\n"
    "v_cmp_ne_u64 vcc, s[64:65], v[160:161]\n"
    "s_cmp_eq_u64 vcc, 0\n"
    "s_cbranch_scc1 .L33_%=\n"
    "s_mov_b32 s98, 100000\n"
    ".L35_%=:\n"
    "buffer_load_dwordx2 v[160:161], v163, s[52:55], s29 offen sc1\n"
    "s_waitcnt vmcnt(0)\n"
    "v_cmp_ne_u64 vcc, s[64:65], v[160:161]\n"
    "s_cmp_eq_u64 vcc, 0\n"
    "s_cbranch_scc1 .L33_%=\n"
    "s_sub_u32 s98, s98, 1\n"
    "s_cbranch_scc0 .L35_%=\n"
    ".L33_%=:\n"
    "v_mfma_f32_16x16x32_bf16 v[88:91], a[12:15], v[120:123], v[88:91]\n"
    "ds_read_b128 v[136:139], v171 offset:6144\n"
    "v_cvt_pk_bf16_f32 v9, v9, v9\n"
    "v_and_b32 v9, 0xffff0000, v9\n"
    "v_mfma_f32_16x16x32_bf16 v[92:95], a[16:19], v[120:123], v[92:95]\n"
    "ds_read_b128 v[140:143], v173 offset:6144\n"
    "v_sqrt_f32 v9, v9\n"
    "v_mfma_f32_16x16x32_bf16 v[96:99], a[20:23], v[120:123], v[96:99]\n"
    "buffer_load_dwordx4 v[24:27], v162, s[52:55], s16 offen sc1\n"
    "v_cvt_pk_bf16_f32 v9, v9, v9\n"
    "v_mfma_f32_16x16x32_bf16 v[100:103], a[12:15], v[124:127], v[100:103]\n"
    "ds_write_b128 v178, v[208:211] offset:12288\n"
    "v_mfma_f32_16x16x32_bf16 v[104:107], a[16:19], v[124:127], v[104:107]\n"
    "v_and_b32 v9, 0xffff0000, v9\n"
    "v_rcp_f32 v10, v9\n"
    "v_mfma_f32_16x16x32_bf16 v[108:111], a[20:23], v[124:127], v[108:111]\n"
    "s_waitcnt lgkmcnt(6)\n"
    "buffer_load_dwordx4 v[28:31], v162, s[52:55], s17 offen sc1\n"
    "v_mul_f32 v15, v9, v9\n"
    "v_mfma_f32_16x16x32_bf16 v[16:19], v[128:131], v[128:131], v[16:19]\n"
    "ds_read_b128 v[120:123], v170 offset:8192\n"
    "v_mul_f32 v11, v10, v10\n"
    "v_mul_f32 v13, 0xc0135761, v10\n"
    "v_mfma_f32_16x16x32_bf16 v[20:23], v[144:147], v[128:131], v[20:23]\n"
    "ds_read_b128 v[124:127], v172 offset:8192\n"
    "v_mul_f32 v11, 0x3d372713, v11\n"
    "v_mul_f32 v14, v11, v13\n"
    "v_mfma_f32_16x16x32_bf16 v[88:91], a[24:27], v[128:131], v[88:91]\n"
    "buffer_load_dwordx4 v[32:35], v162, s[52:55], s18 offen sc1\n"
    "v_lshrrev_b32 v163, 4, v1\n"
    "v_mfma_f32_16x16x32_bf16 v[92:95], a[28:31], v[128:131], v[92:95]\n"
    "v_permlane32_swap_b32 v64, v76\n"
    "v_lshl_add_u32 v163, v163, 7, s41\n"
    "v_mfma_f32_16x16x32_bf16 v[96:99], a[32:35], v[128:131], v[96:99]\n"
    "s_waitcnt lgkmcnt(7)\n"
    "s_waitcnt vmcnt(5)\n"
    "ds_write_b128 v178, v[212:215] offset:16384\n"
    "v_mfma_f32_16x16x32_bf16 v[100:103], a[24:27], v[132:135], v[100:103]\n"
    "buffer_load_dwordx4 v[36:39], v162, s[52:55], s19 offen sc1\n"
    "v_mfma_f32_16x16x32_bf16 v[104:107], a[28:31], v[132:135], v[104:107]\n"
    "v_permlane32_swap_b32 v65, v77\n"
    "v_mfma_f32_16x16x32_bf16 v[108:111], a[32:35], v[132:135], v[108:111]\n"
    "s_waitcnt lgkmcnt(5)\n"
    "v_permlane32_swap_b32 v66, v78\n"
    "v_mfma_f32_16x16x32_bf16 v[88:91], a[36:39], v[136:139], v[88:91]\n"
    "ds_read_b128 v[128:131], v171 offset:10240\n"
    "v_permlane32_swap_b32 v67, v79\n"
    "v_mfma_f32_16x16x32_bf16 v[92:95], a[40:43], v[136:139], v[92:95]\n"
    "ds_read_b128 v[132:135], v173 offset:10240\n"
    "v_permlane32_swap_b32 v68, v80\n"
    "v_mfma_f32_16x16x32_bf16 v[96:99], a[44:47], v[136:139], v[96:99]\n"
    "s_waitcnt lgkmcnt(6)\n"
    "buffer_load_dwordx4 v[40:43], v162, s[52:55], s20 offen sc1\n"
    "v_fmac_f32 v64, v12, v148\n"
    "v_mfma_f32_16x16x32_bf16 v[100:103], a[36:39], v[140:143], v[100:103]\n"
    "s_waitcnt vmcnt(6)\n"
    "ds_write_b128 v178, v[216:219] offset:20480\n"
    "v_mfma_f32_16x16x32_bf16 v[104:107], a[40:43], v[140:143], v[104:107]\n"
    "v_permlane32_swap_b32 v69, v81\n"
    "v_fmac_f32 v65, v12, v149\n"
    "v_mfma_f32_16x16x32_bf16 v[108:111], a[44:47], v[140:143], v[108:111]\n"
    "s_waitcnt lgkmcnt(5)\n"
    "buffer_load_dwordx4 v[44:47], v162, s[52:55], s21 offen sc1\n"
    "v_fmac_f32 v66, v12, v150\n"
    "v_mfma_f32_16x16x32_bf16 v[16:19], v[120:123], v[120:123], v[16:19]\n"
    "ds_read_b128 v[136:139], v170 offset:12288\n"
    "v_permlane32_swap_b32 v70, v82\n"
    "v_mfma_f32_16x16x32_bf16 v[20:23], v[144:147], v[120:123], v[20:23]\n"
    "ds_read_b128 v[140:143], v172 offset:12288\n"
    "v_permlane32_swap_b32 v71, v83\n"
    "v_mfma_f32_16x16x32_bf16 v[88:91], a[48:51], v[120:123], v[88:91]\n"
    "buffer_load_dwordx4 v[48:51], v162, s[52:55], s22 offen sc1\n"
    "v_fmac_f32 v67, v12, v151\n"
    "v_mfma_f32_16x16x32_bf16 v[92:95], a[52:55], v[120:123], v[92:95]\n"
    "s_waitcnt vmcnt(7)\n"
    "ds_write_b128 v178, v[220:223] offset:24576\n"
    "v_mfma_f32_16x16x32_bf16 v[96:99], a[56:59], v[120:123], v[96:99]\n"
    "s_waitcnt lgkmcnt(7)\n"
    "v_permlane32_swap_b32 v72, v84\n"
    "v_mul_f32 v8, v64, v64\n"
    "v_mfma_f32_16x16x32_bf16 v[100:103], a[48:51], v[124:127], v[100:103]\n"
    "buffer_load_dwordx4 v[52:55], v162, s[52:55], s23 offen sc1\n"
    "v_mul_f32 v9, v65, v65\n"
    "v_mfma_f32_16x16x32_bf16 v[104:107], a[52:55], v[124:127], v[104:107]\n"
    "v_permlane32_swap_b32 v73, v85\n"
    "v_mul_f32 v10, v66, v66\n"
    "v_mfma_f32_16x16x32_bf16 v[108:111], a[56:59], v[124:127], v[108:111]\n"
    "s_waitcnt lgkmcnt(5)\n"
    "v_permlane32_swap_b32 v74, v86\n"
    "v_mul_f32 v11, v67, v67\n"
    "v_mfma_f32_16x16x32_bf16 v[88:91], a[60:63], v[128:131], v[88:91]\n"
    "ds_read_b128 v[120:123], v171 offset:14336\n"
    "v_permlane32_swap_b32 v75, v87\n"
    "v_mfma_f32_16x16x32_bf16 v[92:95], a[64:67], v[128:131], v[92:95]\n"
    "ds_read_b128 v[124:127], v173 offset:14336\n"
    "v_fma_f32 v8, v8, v14, v13\n"
    "v_fma_f32 v9, v9, v14, v13\n"
    "v_mfma_f32_16x16x32_bf16 v[96:99], a[68:71], v[128:131], v[96:99]\n"
    "s_waitcnt lgkmcnt(6)\n"
    "buffer_load_dwordx4 v[56:59], v162, s[52:55], s24 offen sc1\n"
    "v_fma_f32 v10, v10, v14, v13\n"
    "v_mfma_f32_16x16x32_bf16 v[100:103], a[60:63], v[132:135], v[100:103]\n"
    "v_fma_f32 v11, v11, v14, v13\n"
    "v_mul_f32 v8, v8, v64\n"
    "v_mul_f32 v9, v9, v65\n"
    "v_mfma_f32_16x16x32_bf16 v[104:107], a[64:67], v[132:135], v[104:107]\n"
    "v_mul_f32 v10, v10, v66\n"
    "v_mul_f32 v11, v11, v67\n"
    "v_fmac_f32 v80, v12, v184\n"
    "v_mfma_f32_16x16x32_bf16 v[108:111], a[68:71], v[132:135], v[108:111]\n"
    "s_waitcnt lgkmcnt(4)\n"
    "buffer_load_dwordx4 v[60:63], v162, s[52:55], s25 offen sc1\n"
    "v_fmac_f32 v81, v12, v185\n"
    "v_mfma_f32_16x16x32_bf16 v[16:19], v[136:139], v[136:139], v[16:19]\n"
    "ds_read_b128 v[128:131], v170 offset:16384\n"
    "v_exp_f32 v8, v8\n"
    "v_mfma_f32_16x16x32_bf16 v[20:23], v[144:147], v[136:139], v[20:23]\n"
    "ds_read_b128 v[132:135], v172 offset:16384\n"
    "v_exp_f32 v9, v9\n"
    "v_mfma_f32_16x16x32_bf16 v[88:91], a[72:75], v[136:139], v[88:91]\n"
    "s_add_u32 s43, s39, 53248\n"
    "v_exp_f32 v10, v10\n"
    "v_fmac_f32 v82, v12, v186\n"
    "v_mfma_f32_16x16x32_bf16 v[92:95], a[76:79], v[136:139], v[92:95]\n"
    "buffer_load_dwordx4 v[224:227], v165, s[52:55], s43 offen offset:0 sc1\n"
    "v_fmac_f32 v83, v12, v187\n"
    "v_mfma_f32_16x16x32_bf16 v[96:99], a[80:83], v[136:139], v[96:99]\n"
    "s_waitcnt lgkmcnt(5)\n"
    "v_exp_f32 v11, v11\n"
    "v_fma_f32 v8, v8, v15, v15\n"
    "v_mfma_f32_16x16x32_bf16 v[100:103], a[72:75], v[140:143], v[100:103]\n"
    "v_fma_f32 v9, v9, v15, v15\n"
    "v_fma_f32 v10, v10, v15, v15\n"
    "v_fma_f32 v11, v11, v15, v15\n"
    "v_mfma_f32_16x16x32_bf16 v[104:107], a[76:79], v[140:143], v[104:107]\n"
    "buffer_load_dwordx4 v[228:231], v165, s[52:55], s43 offen offset:1024 sc1\n"
    "v_mul_f32 v64, v64, v80\n"
    "v_mfma_f32_16x16x32_bf16 v[108:111], a[80:83], v[140:143], v[108:111]\n"
    "s_waitcnt lgkmcnt(3)\n"
    "v_rcp_f32 v8, v8\n"
    "v_mul_f32 v65, v65, v81\n"
    "v_mfma_f32_16x16x32_bf16 v[88:91], a[84:87], v[120:123], v[88:91]\n"
    "ds_read_b128 v[136:139], v171 offset:18432\n"
    "v_rcp_f32 v9, v9\n"
    "v_mfma_f32_16x16x32_bf16 v[92:95], a[88:91], v[120:123], v[92:95]\n"
    "ds_read_b128 v[140:143], v173 offset:18432\n"
    "v_rcp_f32 v10, v10\n"
    "v_mfma_f32_16x16x32_bf16 v[96:99], a[92:95], v[120:123], v[96:99]\n"
    "s_waitcnt lgkmcnt(4)\n"
    "s_waitcnt vmcnt(11)\n"
    "ds_write2st64_b64 v164, v[24:25], v[26:27] offset0:80 offset1:82\n"
    "v_mfma_f32_16x16x32_bf16 v[100:103], a[84:87], v[124:127], v[100:103]\n"
    "buffer_load_dwordx4 v[232:235], v165, s[52:55], s43 offen offset:2048 sc1\n"
    "v_mul_f32 v66, v66, v82\n"
    "v_mfma_f32_16x16x32_bf16 v[104:107], a[88:91], v[124:127], v[104:107]\n"
    "v_rcp_f32 v11, v11\n"
    "v_mul_f32 v67, v67, v83\n"
    "v_mfma_f32_16x16x32_bf16 v[108:111], a[92:95], v[124:127], v[108:111]\n"
    "s_waitcnt lgkmcnt(4)\n"
    "v_mul_f32 v64, v64, v8\n"
    "v_mul_f32 v65, v65, v9\n"
    "v_mul_f32 v66, v66, v10\n"
    "v_mfma_f32_16x16x32_bf16 v[16:19], v[128:131], v[128:131], v[16:19]\n"
    "ds_read_b128 v[120:123], v170 offset:20480\n"
    "v_mul_f32 v67, v67, v11\n"
    "v_cvt_pk_bf16_f32 v2, v64, v65\n"
    "v_mfma_f32_16x16x32_bf16 v[20:23], v[144:147], v[128:131], v[20:23]\n"
    "ds_read_b128 v[124:127], v172 offset:20480\n"
    "v_cvt_pk_bf16_f32 v3, v66, v67\n"
    "v_add_u32 v162, 40960, v162\n"
    "v_mfma_f32_16x16x32_bf16 v[88:91], a[96:99], v[128:131], v[88:91]\n"
    "s_waitcnt vmcnt(11)\n"
    "ds_write2st64_b64 v164, v[28:29], v[30:31] offset0:88 offset1:90\n"
    "v_mfma_f32_16x16x32_bf16 v[92:95], a[100:103], v[128:131], v[92:95]\n"
    "buffer_load_dwordx4 v[236:239], v165, s[52:55], s43 offen offset:3072 sc1\n"
    "v_mfma_f32_16x16x32_bf16 v[96:99], a[104:107], v[128:131], v[96:99]\n"
    "s_waitcnt lgkmcnt(6)\n"
    "s_add_u32 s9, s38, 0\n"
    "v_fmac_f32 v68, v12, v152\n"
    "v_fmac_f32 v69, v12, v153\n"
    "v_fmac_f32 v70, v12, v154\n"
    "v_mfma_f32_16x16x32_bf16 v[100:103], a[96:99], v[132:135], v[100:103]\n"
    "s_waitcnt vmcnt(11)\n"
    "ds_write2st64_b64 v164, v[32:33], v[34:35] offset0:96 offset1:98\n"
    "v_mfma_f32_16x16x32_bf16 v[104:107], a[100:103], v[132:135], v[104:107]\n"
    "s_add_u32 s43, s39, 57344\n"
    "v_fmac_f32 v71, v12, v155\n"
    "v_mul_f32 v8, v68, v68\n"
    "v_mul_f32 v9, v69, v69\n"
    "v_mfma_f32_16x16x32_bf16 v[108:111], a[104:107], v[132:135], v[108:111]\n"
    "s_waitcnt lgkmcnt(6)\n"
    "buffer_load_dwordx4 v[240:243], v165, s[52:55], s43 offen offset:0 sc1\n"
    "v_mul_f32 v10, v70, v70\n"
    "v_mfma_f32_16x16x32_bf16 v[88:91], a[108:111], v[136:139], v[88:91]\n"
    "ds_read_b128 v[128:131], v171 offset:22528\n"
    "v_mul_f32 v11, v71, v71\n"
    "v_fma_f32 v8, v8, v14, v13\n"
    "v_mfma_f32_16x16x32_bf16 v[92:95], a[112:115], v[136:139], v[92:95]\n"
    "ds_read_b128 v[132:135], v173 offset:22528\n"
    "v_fma_f32 v9, v9, v14, v13\n"
    "v_fma_f32 v10, v10, v14, v13\n"
    "v_mfma_f32_16x16x32_bf16 v[96:99], a[116:119], v[136:139], v[96:99]\n"
    "s_waitcnt lgkmcnt(7)\n"
    "s_waitcnt vmcnt(11)\n"
    "ds_write2st64_b64 v164, v[36:37], v[38:39] offset0:104 offset1:106\n"
    "v_mfma_f32_16x16x32_bf16 v[100:103], a[108:111], v[140:143], v[100:103]\n"
    "buffer_load_dwordx4 v[244:247], v165, s[52:55], s43 offen offset:1024 sc1\n"
    "v_fma_f32 v11, v11, v14, v13\n"
    "v_mfma_f32_16x16x32_bf16 v[104:107], a[112:115], v[140:143], v[104:107]\n"
    "v_mul_f32 v8, v8, v68\n"
    "v_mul_f32 v9, v9, v69\n"
    "v_mul_f32 v10, v10, v70\n"
    "v_mfma_f32_16x16x32_bf16 v[108:111], a[116:119], v[140:143], v[108:111]\n"
    "s_waitcnt lgkmcnt(6)\n"
    "v_mul_f32 v11, v11, v71\n"
    "v_exp_f32 v8, v8\n"
    "v_mfma_f32_16x16x32_bf16 v[16:19], v[120:123], v[120:123], v[16:19]\n"
    "ds_read_b128 v[136:139], v170 offset:24576\n"
    "v_exp_f32 v9, v9\n"
    "v_mfma_f32_16x16x32_bf16 v[20:23], v[144:147], v[120:123], v[20:23]\n"
    "ds_read_b128 v[140:143], v172 offset:24576\n"
    "v_exp_f32 v10, v10\n"
    "v_mfma_f32_16x16x32_bf16 v[88:91], a[120:123], v[120:123], v[88:91]\n"
    "s_waitcnt vmcnt(11)\n"
    "ds_write2st64_b64 v164, v[40:41], v[42:43] offset0:112 offset1:114\n"
    "v_mfma_f32_16x16x32_bf16 v[92:95], a[124:127], v[120:123], v[92:95]\n"
    "buffer_load_dwordx4 v[248:251], v165, s[52:55], s43 offen offset:2048 sc1\n"
    "v_fmac_f32 v76, v12, v188\n"
    "v_mfma_f32_16x16x32_bf16 v[96:99], a[128:131], v[120:123], v[96:99]\n"
    "s_waitcnt lgkmcnt(8)\n"
    "v_exp_f32 v11, v11\n"
    "v_fmac_f32 v77, v12, v189\n"
    "v_mfma_f32_16x16x32_bf16 v[100:103], a[120:123], v[124:127], v[100:103]\n"
    "s_waitcnt vmcnt(11)\n"
    "ds_write2st64_b64 v164, v[44:45], v[46:47] offset0:120 offset1:122\n"
    "v_mfma_f32_16x16x32_bf16 v[104:107], a[124:127], v[124:127], v[104:107]\n"
    "buffer_load_dwordx4 v[252:255], v165, s[52:55], s43 offen offset:3072 sc1\n"
    "v_fmac_f32 v78, v12, v190\n"
    "v_mfma_f32_16x16x32_bf16 v[108:111], a[128:131], v[124:127], v[108:111]\n"
    "s_waitcnt lgkmcnt(6)\n"
    "s_add_u32 s43, s39, 15360\n"
    "v_fmac_f32 v79, v12, v191\n"
    "v_fma_f32 v8, v8, v15, v15\n"
    "v_fma_f32 v9, v9, v15, v15\n"
    "v_mfma_f32_16x16x32_bf16 v[88:91], a[132:135], v[128:131], v[88:91]\n"
    "ds_read_b128 v[120:123], v171 offset:26624\n"
    "v_fma_f32 v10, v10, v15, v15\n"
    "v_fma_f32 v11, v11, v15, v15\n"
    "v_mfma_f32_16x16x32_bf16 v[92:95], a[136:139], v[128:131], v[92:95]\n"
    "ds_read_b128 v[124:127], v173 offset:26624\n"
    "v_rcp_f32 v8, v8\n"
    "v_mfma_f32_16x16x32_bf16 v[96:99], a[140:143], v[128:131], v[96:99]\n"
    "s_waitcnt lgkmcnt(7)\n"
    "s_waitcnt vmcnt(11)\n"
    "ds_write2st64_b64 v164, v[48:49], v[50:51] offset0:128 offset1:130\n"
    "v_mfma_f32_16x16x32_bf16 v[100:103], a[132:135], v[132:135], v[100:103]\n"
    "buffer_load_dwordx4 a[240:243], v165, s[52:55], s43 offen offset:0 sc1\n"
    "v_mul_f32 v68, v68, v76\n"
    "v_mfma_f32_16x16x32_bf16 v[104:107], a[136:139], v[132:135], v[104:107]\n"
    "v_rcp_f32 v9, v9\n"
    "v_mul_f32 v69, v69, v77\n"
    "v_mfma_f32_16x16x32_bf16 v[108:111], a[140:143], v[132:135], v[108:111]\n"
    "s_waitcnt lgkmcnt(6)\n"
    "v_rcp_f32 v10, v10\n"
    "v_mul_f32 v70, v70, v78\n"
    "v_mfma_f32_16x16x32_bf16 v[16:19], v[136:139], v[136:139], v[16:19]\n"
    "ds_read_b128 v[128:131], v170 offset:28672\n"
    "v_rcp_f32 v11, v11\n"
    "v_mfma_f32_16x16x32_bf16 v[20:23], v[144:147], v[136:139], v[20:23]\n"
    "ds_read_b128 v[132:135], v172 offset:28672\n"
    "v_mul_f32 v71, v71, v79\n"
    "v_mul_f32 v68, v68, v8\n"
    "v_mfma_f32_16x16x32_bf16 v[88:91], a[144:147], v[136:139], v[88:91]\n"
    "s_waitcnt vmcnt(11)\n"
    "ds_write2st64_b64 v164, v[52:53], v[54:55] offset0:136 offset1:138\n"
    "v_mfma_f32_16x16x32_bf16 v[92:95], a[148:151], v[136:139], v[92:95]\n"
    "buffer_load_dwordx4 a[244:247], v165, s[52:55], s43 offen offset:1024 sc1\n"
    "v_mul_f32 v69, v69, v9\n"
    "v_mfma_f32_16x16x32_bf16 v[96:99], a[152:155], v[136:139], v[96:99]\n"
    "s_waitcnt lgkmcnt(8)\n"
    "v_mul_f32 v70, v70, v10\n"
    "v_mul_f32 v71, v71, v11\n"
    "v_cvt_pk_bf16_f32 v4, v68, v69\n"
    "v_mfma_f32_16x16x32_bf16 v[100:103], a[144:147], v[140:143], v[100:103]\n"
    "s_waitcnt vmcnt(11)\n"
    "ds_write2st64_b64 v164, v[56:57], v[58:59] offset0:144 offset1:146\n"
    "v_mfma_f32_16x16x32_bf16 v[104:107], a[148:151], v[140:143], v[104:107]\n"
    "buffer_load_dwordx4 a[248:251], v165, s[52:55], s43 offen offset:2048 sc1\n"
    "v_cvt_pk_bf16_f32 v5, v70, v71\n"
    "v_mfma_f32_16x16x32_bf16 v[108:111], a[152:155], v[140:143], v[108:111]\n"
    "s_waitcnt lgkmcnt(6)\n"
    "v_mfma_f32_16x16x32_bf16 v[88:91], a[156:159], v[120:123], v[88:91]\n"
    "ds_read_b128 v[136:139], v171 offset:30720\n"
    "v_mfma_f32_16x16x32_bf16 v[92:95], a[160:163], v[120:123], v[92:95]\n"
    "ds_read_b128 v[140:143], v173 offset:30720\n"
    "v_mfma_f32_16x16x32_bf16 v[96:99], a[164:167], v[120:123], v[96:99]\n"
    "s_waitcnt lgkmcnt(7)\n"
    "buffer_store_dwordx4 v[2:5], v176, s[52:55], s9 offen sc1\n"
    "v_mfma_f32_16x16x32_bf16 v[100:103], a[156:159], v[124:127], v[100:103]\n"
    "s_waitcnt vmcnt(12)\n"
    "ds_write2st64_b64 v164, v[60:61], v[62:63] offset0:152 offset1:154\n"
    "v_mfma_f32_16x16x32_bf16 v[104:107], a[160:163], v[124:127], v[104:107]\n"
    "v_fmac_f32 v72, v12, v156\n"
    "v_fmac_f32 v73, v12, v157\n"
    "v_fmac_f32 v74, v12, v158\n"
    "v_mfma_f32_16x16x32_bf16 v[108:111], a[164:167], v[124:127], v[108:111]\n"
    "s_waitcnt lgkmcnt(6)\n"
    "buffer_load_dwordx4 a[252:255], v165, s[52:55], s43 offen offset:3072 sc1\n"
    "v_fmac_f32 v75, v12, v159\n"
    "v_mfma_f32_16x16x32_bf16 v[16:19], v[128:131], v[128:131], v[16:19]\n"
    "ds_read_b128 v[120:123], v170 offset:32768\n"
    "v_mul_f32 v8, v72, v72\n"
    "v_mul_f32 v9, v73, v73\n"
    "v_mfma_f32_16x16x32_bf16 v[20:23], v[144:147], v[128:131], v[20:23]\n"
    "ds_read_b128 v[124:127], v172 offset:32768\n"
    "v_mul_f32 v10, v74, v74\n"
    "v_mul_f32 v11, v75, v75\n"
    "v_mfma_f32_16x16x32_bf16 v[88:91], a[168:171], v[128:131], v[88:91]\n"
    "s_waitcnt vmcnt(12)\n"
    "ds_write_b128 v178, v[224:227] offset:28672\n"
    "v_mfma_f32_16x16x32_bf16 v[92:95], a[172:175], v[128:131], v[92:95]\n"
    "s_add_u32 s43, s39, 0\n"
    "v_fma_f32 v8, v8, v14, v13\n"
    "v_fma_f32 v9, v9, v14, v13\n"
    "v_fma_f32 v10, v10, v14, v13\n"
    "v_mfma_f32_16x16x32_bf16 v[96:99], a[176:179], v[128:131], v[96:99]\n"
    "s_waitcnt lgkmcnt(8)\n"
    "buffer_load_dwordx4 v[196:199], v165, s[52:55], s43 offen offset:0 sc1\n"
    "v_fma_f32 v11, v11, v14, v13\n"
    "v_mfma_f32_16x16x32_bf16 v[100:103], a[168:171], v[132:135], v[100:103]\n"
    "s_waitcnt vmcnt(12)\n"
    "ds_write_b128 v178, v[228:231] offset:32768\n"
    "v_mfma_f32_16x16x32_bf16 v[104:107], a[172:175], v[132:135], v[104:107]\n"
    "v_mul_f32 v8, v8, v72\n"
    "v_mul_f32 v9, v9, v73\n"
    "v_mul_f32 v10, v10, v74\n"
    "v_mfma_f32_16x16x32_bf16 v[108:111], a[176:179], v[132:135], v[108:111]\n"
    "s_waitcnt lgkmcnt(6)\n"
    "buffer_load_dwordx4 v[200:203], v165, s[52:55], s43 offen offset:1024 sc1\n"
    "v_mul_f32 v11, v11, v75\n"
    "v_mfma_f32_16x16x32_bf16 v[88:91], a[180:183], v[136:139], v[88:91]\n"
    "ds_read_b128 v[128:131], v171 offset:34816\n"
    "v_exp_f32 v8, v8\n"
    "v_mfma_f32_16x16x32_bf16 v[92:95], a[184:187], v[136:139], v[92:95]\n"
    "ds_read_b128 v[132:135], v173 offset:34816\n"
    "v_exp_f32 v9, v9\n"
    "v_mfma_f32_16x16x32_bf16 v[96:99], a[188:191], v[136:139], v[96:99]\n"
    "s_waitcnt lgkmcnt(7)\n"
    "buffer_load_dwordx4 v[204:207], v165, s[52:55], s43 offen offset:2048 sc1\n"
    "v_fmac_f32 v84, v12, v192\n"
    "v_mfma_f32_16x16x32_bf16 v[100:103], a[180:183], v[140:143], v[100:103]\n"
    "s_waitcnt vmcnt(13)\n"
    "ds_write_b128 v178, v[232:235] offset:36864\n"
    "v_mfma_f32_16x16x32_bf16 v[104:107], a[184:187], v[140:143], v[104:107]\n"
    "v_exp_f32 v10, v10\n"
    "v_fmac_f32 v85, v12, v193\n"
    "v_mfma_f32_16x16x32_bf16 v[108:111], a[188:191], v[140:143], v[108:111]\n"
    "s_waitcnt lgkmcnt(6)\n"
    "buffer_load_dwordx4 v[208:211], v165, s[52:55], s43 offen offset:3072 sc1\n"
    "v_fmac_f32 v86, v12, v194\n"
    "v_mfma_f32_16x16x32_bf16 v[16:19], v[120:123], v[120:123], v[16:19]\n"
    "ds_read_b128 v[136:139], v170 offset:36864\n"
    "v_exp_f32 v11, v11\n"
    "v_mfma_f32_16x16x32_bf16 v[20:23], v[144:147], v[120:123], v[20:23]\n"
    "ds_read_b128 v[140:143], v172 offset:36864\n"
    "v_fmac_f32 v87, v12, v195\n"
    "v_fma_f32 v8, v8, v15, v15\n"
    "v_mfma_f32_16x16x32_bf16 v[88:91], a[192:195], v[120:123], v[88:91]\n"
    "buffer_load_dwordx4 v[24:27], v162, s[52:55], s16 offen sc1\n"
    "v_fma_f32 v9, v9, v15, v15\n"
    "v_mfma_f32_16x16x32_bf16 v[92:95], a[196:199], v[120:123], v[92:95]\n"
    "s_waitcnt vmcnt(14)\n"
    "ds_write_b128 v178, v[236:239] offset:40960\n"
    "v_mfma_f32_16x16x32_bf16 v[96:99], a[200:203], v[120:123], v[96:99]\n"
    "s_waitcnt lgkmcnt(8)\n"
    "v_fma_f32 v10, v10, v15, v15\n"
    "v_fma_f32 v11, v11, v15, v15\n"
    "v_mul_f32 v72, v72, v84\n"
    "v_mfma_f32_16x16x32_bf16 v[100:103], a[192:195], v[124:127], v[100:103]\n"
    "v_rcp_f32 v8, v8\n"
    "v_mul_f32 v73, v73, v85\n"
    "v_mfma_f32_16x16x32_bf16 v[104:107], a[196:199], v[124:127], v[104:107]\n"
    "buffer_load_dwordx4 v[28:31], v162, s[52:55], s17 offen sc1\n"
    "v_mul_f32 v74, v74, v86\n"
    "v_mfma_f32_16x16x32_bf16 v[108:111], a[200:203], v[124:127], v[108:111]\n"
    "s_waitcnt lgkmcnt(5)\n"
    "v_rcp_f32 v9, v9\n"
    "v_mul_f32 v75, v75, v87\n"
    "v_mfma_f32_16x16x32_bf16 v[88:91], a[204:207], v[128:131], v[88:91]\n"
    "ds_read_b128 v[120:123], v171 offset:38912\n"
    "v_rcp_f32 v10, v10\n"
    "v_mfma_f32_16x16x32_bf16 v[92:95], a[208:211], v[128:131], v[92:95]\n"
    "ds_read_b128 v[124:127], v173 offset:38912\n"
    "v_rcp_f32 v11, v11\n"
    "v_mfma_f32_16x16x32_bf16 v[96:99], a[212:215], v[128:131], v[96:99]\n"
    "s_waitcnt lgkmcnt(6)\n"
    "buffer_load_dwordx4 v[32:35], v162, s[52:55], s18 offen sc1\n"
    "v_mul_f32 v72, v72, v8\n"
    "v_mfma_f32_16x16x32_bf16 v[100:103], a[204:207], v[132:135], v[100:103]\n"
    "s_waitcnt vmcnt(15)\n"
    "ds_write_b128 v178, v[240:243] offset:45056\n"
    "v_mfma_f32_16x16x32_bf16 v[104:107], a[208:211], v[132:135], v[104:107]\n"
    "v_mul_f32 v73, v73, v9\n"
    "v_mul_f32 v74, v74, v10\n"
    "v_mul_f32 v75, v75, v11\n"
    "v_mfma_f32_16x16x32_bf16 v[108:111], a[212:215], v[132:135], v[108:111]\n"
    "s_waitcnt lgkmcnt(5)\n"
    "v_cvt_pk_bf16_f32 v6, v72, v73\n"
    "v_cvt_pk_bf16_f32 v7, v74, v75\n"
    "v_mfma_f32_16x16x32_bf16 v[16:19], v[136:139], v[136:139], v[16:19]\n"
    "buffer_store_dwordx2 v[6:7], v177, s[52:55], s9 offen offset:2048 sc1\n"
    "v_mfma_f32_16x16x32_bf16 v[20:23], v[144:147], v[136:139], v[20:23]\n"
    "s_waitcnt vmcnt(15)\n"
    "ds_write_b128 v178, v[244:247] offset:49152\n"
    "v_mfma_f32_16x16x32_bf16 v[88:91], a[216:219], v[136:139], v[88:91]\n"
    "v_mfma_f32_16x16x32_bf16 v[92:95], a[220:223], v[136:139], v[92:95]\n"
    "v_mfma_f32_16x16x32_bf16 v[96:99], a[224:227], v[136:139], v[96:99]\n"
    "s_waitcnt lgkmcnt(5)\n"
    "v_mfma_f32_16x16x32_bf16 v[100:103], a[216:219], v[140:143], v[100:103]\n"
    "s_nop 0\n"
    "v_cndmask_b32 v21, v16, v17, s[32:33]\n"
    "v_cndmask_b32 v21, v21, v18, s[34:35]\n"
    "v_mfma_f32_16x16x32_bf16 v[104:107], a[220:223], v[140:143], v[104:107]\n"
    "v_cndmask_b32 v21, v21, v19, s[36:37]\n"
    "ds_write_b64 v174, v[20:21] offset:2048\n"
    "v_mfma_f32_16x16x32_bf16 v[108:111], a[224:227], v[140:143], v[108:111]\n"
    "s_waitcnt lgkmcnt(4)\n"
    "s_waitcnt lgkmcnt(0)\n"
    "s_barrier\n"
    "ds_read_b128 v[128:131], v166 offset:0\n"
    "ds_read_b128 v[132:135], v168 offset:0\n"
    "v_mfma_f32_16x16x32_bf16 v[88:91], a[228:231], v[120:123], v[88:91]\n"
    "ds_read_b128 v[136:139], v167 offset:2048\n"
    "v_mfma_f32_16x16x32_bf16 v[92:95], a[232:235], v[120:123], v[92:95]\n"
    "ds_read_b128 v[140:143], v169 offset:2048\n"
    "v_mfma_f32_16x16x32_bf16 v[96:99], a[236:239], v[120:123], v[96:99]\n"
    "buffer_load_dwordx4 v[36:39], v162, s[52:55], s19 offen sc1\n"
    "v_mfma_f32_16x16x32_bf16 v[100:103], a[228:231], v[124:127], v[100:103]\n"
    "ds_read_b128 v[8:11], v175 offset:2048\n"
    "v_mfma_f32_16x16x32_bf16 v[104:107], a[232:235], v[124:127], v[104:107]\n"
    "v_mfma_f32_16x16x32_bf16 v[108:111], a[236:239], v[124:127], v[108:111]\n"
    "s_waitcnt lgkmcnt(4)\n"
    "buffer_load_dwordx4 v[40:43], v162, s[52:55], s20 offen sc1\n"
    "v_mfma_f32_16x16x32_bf16 v[16:19], v[128:131], v[128:131], 0\n"
    "ds_read_b128 v[120:123], v166 offset:4096\n"
    "v_mfma_f32_16x16x32_bf16 v[20:23], v[144:147], v[128:131], 0\n"
    "ds_read_b128 v[124:127], v168 offset:4096\n"
    "v_mfma_f32_16x16x32_bf16 v[64:67], a[0:3], v[128:131], 0\n"
    "buffer_load_dwordx4 v[44:47], v162, s[52:55], s21 offen sc1\n"
    "v_mfma_f32_16x16x32_bf16 v[68:71], a[4:7], v[128:131], 0\n"
    "s_waitcnt vmcnt(17)\n"
    "ds_write_b128 v178, v[248:251] offset:53248\n"
    "v_mfma_f32_16x16x32_bf16 v[72:75], a[8:11], v[128:131], 0\n"
    "s_waitcnt lgkmcnt(6)\n"
    "s_waitcnt lgkmcnt(3)\n"
    "v_add_f32 v8, v8, v10\n"
    "v_add_f32 v9, v9, v11\n"
    "v_mul_f32 v8, 0x3acccccd, v8\n"
    "v_mfma_f32_16x16x32_bf16 v[76:79], a[0:3], v[132:135], 0\n"
    "buffer_load_dwordx4 v[48:51], v162, s[52:55], s22 offen sc1\n"
    "v_mul_f32 v9, 0x3acccccd, v9\n"
    "v_mfma_f32_16x16x32_bf16 v[80:83], a[4:7], v[132:135], 0\n"
    "v_fma_f32 v9, -v8, v8, v9\n"
    "v_max_f32 v9, 0, v9\n"
    "v_cvt_pk_bf16_f32 v8, -v8, v9\n"
    "v_mfma_f32_16x16x32_bf16 v[84:87], a[8:11], v[132:135], 0\n"
    "v_and_b32 v9, 0xffff0000, v8\n"
    "v_lshlrev_b32 v112, 16, v8\n"
    "v_add_f32 v9, 0x3727c5ac, v9\n"
    "v_mfma_f32_16x16x32_bf16 v[64:67], a[12:15], v[136:139], v[64:67]\n"
    "ds_read_b128 v[128:131], v167 offset:6144\n"
    "v_cvt_pk_bf16_f32 v9, v9, v9\n"
    "v_and_b32 v9, 0xffff0000, v9\n"
    "v_mfma_f32_16x16x32_bf16 v[68:71], a[16:19], v[136:139], v[68:71]\n"
    "ds_read_b128 v[132:135], v169 offset:6144\n"
    "v_sqrt_f32 v9, v9\n"
    "v_mfma_f32_16x16x32_bf16 v[72:75], a[20:23], v[136:139], v[72:75]\n"
    "buffer_load_dwordx4 v[52:55], v162, s[52:55], s23 offen sc1\n"
    "v_cvt_pk_bf16_f32 v9, v9, v9\n"
    "v_mfma_f32_16x16x32_bf16 v[76:79], a[12:15], v[140:143], v[76:79]\n"
    "s_waitcnt vmcnt(18)\n"
    "ds_write_b128 v178, v[252:255] offset:57344\n"
    "v_mfma_f32_16x16x32_bf16 v[80:83], a[16:19], v[140:143], v[80:83]\n"
    "v_and_b32 v9, 0xffff0000, v9\n"
    "v_rcp_f32 v10, v9\n"
    "v_mfma_f32_16x16x32_bf16 v[84:87], a[20:23], v[140:143], v[84:87]\n"
    "s_waitcnt lgkmcnt(5)\n"
    "buffer_load_dwordx4 v[56:59], v162, s[52:55], s24 offen sc1\n"
    "v_mul_f32 v115, v9, v9\n"
    "v_mfma_f32_16x16x32_bf16 v[16:19], v[120:123], v[120:123], v[16:19]\n"
    "ds_read_b128 v[136:139], v166 offset:8192\n"
    "v_mul_f32 v11, v10, v10\n"
    "v_mul_f32 v113, 0xc0135761, v10\n"
    "v_mfma_f32_16x16x32_bf16 v[20:23], v[144:147], v[120:123], v[20:23]\n"
    "ds_read_b128 v[140:143], v168 offset:8192\n"
    "v_mul_f32 v11, 0x3d372713, v11\n"
    "v_mul_f32 v114, v11, v113\n"
    "v_mfma_f32_16x16x32_bf16 v[64:67], a[24:27], v[120:123], v[64:67]\n"
    "buffer_load_dwordx4 v[60:63], v162, s[52:55], s25 offen sc1\n"
    "v_mfma_f32_16x16x32_bf16 v[68:71], a[28:31], v[120:123], v[68:71]\n"
    "s_waitcnt vmcnt(10)\n"
    "ds_write2st64_b64 v164, v[24:25], v[26:27] offset0:0 offset1:2\n"
    "v_mfma_f32_16x16x32_bf16 v[72:75], a[32:35], v[120:123], v[72:75]\n"
    "s_waitcnt lgkmcnt(7)\n"
    "s_add_u32 s43, s39, 4096\n"
    "v_permlane32_swap_b32 v88, v100\n"
    "v_mfma_f32_16x16x32_bf16 v[76:79], a[24:27], v[124:127], v[76:79]\n"
    "buffer_load_dwordx4 v[212:215], v165, s[52:55], s43 offen offset:0 sc1\n"
    "v_mfma_f32_16x16x32_bf16 v[80:83], a[28:31], v[124:127], v[80:83]\n"
    "v_permlane32_swap_b32 v89, v101\n"
    "v_mfma_f32_16x16x32_bf16 v[84:87], a[32:35], v[124:127], v[84:87]\n"
    "s_waitcnt lgkmcnt(5)\n"
    "v_permlane32_swap_b32 v90, v102\n"
    "v_mfma_f32_16x16x32_bf16 v[64:67], a[36:39], v[128:131], v[64:67]\n"
    "ds_read_b128 v[120:123], v167 offset:10240\n"
    "v_permlane32_swap_b32 v91, v103\n"
    "v_mfma_f32_16x16x32_bf16 v[68:71], a[40:43], v[128:131], v[68:71]\n"
    "ds_read_b128 v[124:127], v169 offset:10240\n"
    "v_permlane32_swap_b32 v92, v104\n"
    "v_mfma_f32_16x16x32_bf16 v[72:75], a[44:47], v[128:131], v[72:75]\n"
    "s_waitcnt lgkmcnt(6)\n"
    "buffer_load_dwordx4 v[216:219], v165, s[52:55], s43 offen offset:1024 sc1\n"
    "v_fmac_f32 v88, v112, v148\n"
    "v_mfma_f32_16x16x32_bf16 v[76:79], a[36:39], v[132:135], v[76:79]\n"
    "s_waitcnt vmcnt(11)\n"
    "ds_write2st64_b64 v164, v[28:29], v[30:31] offset0:8 offset1:10\n"
    "v_mfma_f32_16x16x32_bf16 v[80:83], a[40:43], v[132:135], v[80:83]\n"
    "v_permlane32_swap_b32 v93, v105\n"
    "v_fmac_f32 v89, v112, v149\n"
    "v_mfma_f32_16x16x32_bf16 v[84:87], a[44:47], v[132:135], v[84:87]\n"
    "s_waitcnt lgkmcnt(5)\n"
    "buffer_load_dwordx4 v[220:223], v165, s[52:55], s43 offen offset:2048 sc1\n"
    "v_fmac_f32 v90, v112, v150\n"
    "v_mfma_f32_16x16x32_bf16 v[16:19], v[136:139], v[136:139], v[16:19]\n"
    "ds_read_b128 v[128:131], v166 offset:12288\n"
    "v_permlane32_swap_b32 v94, v106\n"
    "v_mfma_f32_16x16x32_bf16 v[20:23], v[144:147], v[136:139], v[20:23]\n"
    "ds_read_b128 v[132:135], v168 offset:12288\n"
    "v_permlane32_swap_b32 v95, v107\n"
    "v_mfma_f32_16x16x32_bf16 v[64:67], a[48:51], v[136:139], v[64:67]\n"
    "s_waitcnt vmcnt(11)\n"
    "ds_write2st64_b64 v164, v[32:33], v[34:35] offset0:16 offset1:18\n"
    "v_mfma_f32_16x16x32_bf16 v[68:71], a[52:55], v[136:139], v[68:71]\n"
    "buffer_load_dwordx4 v[224:227], v165, s[52:55], s43 offen offset:3072 sc1\n"
    "v_fmac_f32 v91, v112, v151\n"
    "v_mfma_f32_16x16x32_bf16 v[72:75], a[56:59], v[136:139], v[72:75]\n"
    "s_waitcnt lgkmcnt(7)\n"
    "s_add_u32 s43, s39, 8192\n"
    "v_permlane32_swap_b32 v96, v108\n"
    "v_mul_f32 v8, v88, v88\n"
    "v_mfma_f32_16x16x32_bf16 v[76:79], a[48:51], v[140:143], v[76:79]\n"
    "v_permlane32_swap_b32 v97, v109\n"
    "v_mul_f32 v9, v89, v89\n"
    "v_mfma_f32_16x16x32_bf16 v[80:83], a[52:55], v[140:143], v[80:83]\n"
    "v_permlane32_swap_b32 v98, v110\n"
    "v_mul_f32 v10, v90, v90\n"
    "v_mfma_f32_16x16x32_bf16 v[84:87], a[56:59], v[140:143], v[84:87]\n"
    "s_waitcnt lgkmcnt(5)\n"
    "buffer_load_dwordx4 v[228:231], v165, s[52:55], s43 offen offset:0 sc1\n"
    "v_mul_f32 v11, v91, v91\n"
    "v_mfma_f32_16x16x32_bf16 v[64:67], a[60:63], v[120:123], v[64:67]\n"
    "ds_read_b128 v[136:139], v167 offset:14336\n"
    "v_permlane32_swap_b32 v99, v111\n"
    "v_mfma_f32_16x16x32_bf16 v[68:71], a[64:67], v[120:123], v[68:71]\n"
    "ds_read_b128 v[140:143], v169 offset:14336\n"
    "v_fma_f32 v8, v8, v114, v113\n"
    "v_fma_f32 v9, v9, v114, v113\n"
    "v_mfma_f32_16x16x32_bf16 v[72:75], a[68:71], v[120:123], v[72:75]\n"
    "s_waitcnt lgkmcnt(6)\n"
    "s_waitcnt vmcnt(11)\n"
    "ds_write2st64_b64 v164, v[36:37], v[38:39] offset0:24 offset1:26\n"
    "v_mfma_f32_16x16x32_bf16 v[76:79], a[60:63], v[124:127], v[76:79]\n"
    "buffer_load_dwordx4 v[232:235], v165, s[52:55], s43 offen offset:1024 sc1\n"
    "v_fma_f32 v10, v10, v114, v113\n"
    "v_mfma_f32_16x16x32_bf16 v[80:83], a[64:67], v[124:127], v[80:83]\n"
    "v_fma_f32 v11, v11, v114, v113\n"
    "v_mul_f32 v8, v8, v88\n"
    "v_mul_f32 v9, v9, v89\n"
    "v_mfma_f32_16x16x32_bf16 v[84:87], a[68:71], v[124:127], v[84:87]\n"
    "s_waitcnt lgkmcnt(5)\n"
    "v_mul_f32 v10, v10, v90\n"
    "v_mul_f32 v11, v11, v91\n"
    "v_fmac_f32 v104, v112, v184\n"
    "v_mfma_f32_16x16x32_bf16 v[16:19], v[128:131], v[128:131], v[16:19]\n"
    "ds_read_b128 v[120:123], v166 offset:16384\n"
    "v_exp_f32 v8, v8\n"
    "v_mfma_f32_16x16x32_bf16 v[20:23], v[144:147], v[128:131], v[20:23]\n"
    "ds_read_b128 v[124:127], v168 offset:16384\n"
    "v_exp_f32 v9, v9\n"
    "v_mfma_f32_16x16x32_bf16 v[64:67], a[72:75], v[128:131], v[64:67]\n"
    "buffer_load_dwordx4 v[236:239], v165, s[52:55], s43 offen offset:2048 sc1\n"
    "v_fmac_f32 v105, v112, v185\n"
    "v_mfma_f32_16x16x32_bf16 v[68:71], a[76:79], v[128:131], v[68:71]\n"
    "s_waitcnt vmcnt(12)\n"
    "ds_write2st64_b64 v164, v[40:41], v[42:43] offset0:32 offset1:34\n"
    "v_mfma_f32_16x16x32_bf16 v[72:75], a[80:83], v[128:131], v[72:75]\n"
    "s_waitcnt lgkmcnt(7)\n"
    "v_exp_f32 v10, v10\n"
    "v_fmac_f32 v106, v112, v186\n"
    "v_mfma_f32_16x16x32_bf16 v[76:79], a[72:75], v[132:135], v[76:79]\n"
    "buffer_load_dwordx4 v[240:243], v165, s[52:55], s43 offen offset:3072 sc1\n"
    "v_fmac_f32 v107, v112, v187\n"
    "v_mfma_f32_16x16x32_bf16 v[80:83], a[76:79], v[132:135], v[80:83]\n"
    "s_add_u32 s43, s39, 12288\n"
    "v_exp_f32 v11, v11\n"
    "v_fma_f32 v8, v8, v115, v115\n"
    "v_mfma_f32_16x16x32_bf16 v[84:87], a[80:83], v[132:135], v[84:87]\n"
    "s_waitcnt lgkmcnt(5)\n"
    "v_fma_f32 v9, v9, v115, v115\n"
    "v_fma_f32 v10, v10, v115, v115\n"
    "v_fma_f32 v11, v11, v115, v115\n"
    "v_mfma_f32_16x16x32_bf16 v[64:67], a[84:87], v[136:139], v[64:67]\n"
    "ds_read_b128 v[128:131], v167 offset:18432\n"
    "v_rcp_f32 v8, v8\n"
    "v_mfma_f32_16x16x32_bf16 v[68:71], a[88:91], v[136:139], v[68:71]\n"
    "ds_read_b128 v[132:135], v169 offset:18432\n"
    "v_rcp_f32 v9, v9\n"
    "v_mfma_f32_16x16x32_bf16 v[72:75], a[92:95], v[136:139], v[72:75]\n"
    "s_waitcnt lgkmcnt(6)\n"
    "buffer_load_dwordx4 v[244:247], v165, s[52:55], s43 offen offset:0 sc1\n"
    "v_mul_f32 v88, v88, v104\n"
    "v_mfma_f32_16x16x32_bf16 v[76:79], a[84:87], v[140:143], v[76:79]\n"
    "s_waitcnt vmcnt(13)\n"
    "ds_write2st64_b64 v164, v[44:45], v[46:47] offset0:40 offset1:42\n"
    "v_mfma_f32_16x16x32_bf16 v[80:83], a[88:91], v[140:143], v[80:83]\n"
    "v_rcp_f32 v10, v10\n"
    "v_mul_f32 v89, v89, v105\n"
    "v_mfma_f32_16x16x32_bf16 v[84:87], a[92:95], v[140:143], v[84:87]\n"
    "s_waitcnt lgkmcnt(5)\n"
    "buffer_load_dwordx4 v[248:251], v165, s[52:55], s43 offen offset:1024 sc1\n"
    "v_mul_f32 v90, v90, v106\n"
    "v_mfma_f32_16x16x32_bf16 v[16:19], v[120:123], v[120:123], v[16:19]\n"
    "ds_read_b128 v[136:139], v166 offset:20480\n"
    "v_rcp_f32 v11, v11\n"
    "v_mfma_f32_16x16x32_bf16 v[20:23], v[144:147], v[120:123], v[20:23]\n"
    "ds_read_b128 v[140:143], v168 offset:20480\n"
    "v_mul_f32 v91, v91, v107\n"
    "v_mul_f32 v88, v88, v8\n"
    "v_mfma_f32_16x16x32_bf16 v[64:67], a[96:99], v[120:123], v[64:67]\n"
    "s_waitcnt vmcnt(13)\n"
    "ds_write2st64_b64 v164, v[48:49], v[50:51] offset0:48 offset1:50\n"
    "v_mfma_f32_16x16x32_bf16 v[68:71], a[100:103], v[120:123], v[68:71]\n"
    "buffer_load_dwordx4 v[252:255], v165, s[52:55], s43 offen offset:2048 sc1\n"
    "v_mul_f32 v89, v89, v9\n"
    "v_mfma_f32_16x16x32_bf16 v[72:75], a[104:107], v[120:123], v[72:75]\n"
    "s_waitcnt lgkmcnt(7)\n"
    "s_add_u32 s43, s39, 19456\n"
    "v_mul_f32 v90, v90, v10\n"
    "v_mul_f32 v91, v91, v11\n"
    "v_cvt_pk_bf16_f32 v2, v88, v89\n"
    "v_mfma_f32_16x16x32_bf16 v[76:79], a[96:99], v[124:127], v[76:79]\n"
    "s_waitcnt vmcnt(13)\n"
    "ds_write2st64_b64 v164, v[52:53], v[54:55] offset0:56 offset1:58\n"
    "v_mfma_f32_16x16x32_bf16 v[80:83], a[100:103], v[124:127], v[80:83]\n"
    "v_cvt_pk_bf16_f32 v3, v90, v91\n"
    "s_add_u32 s9, s38, 98304\n"
    "v_fmac_f32 v92, v112, v152\n"
    "v_mfma_f32_16x16x32_bf16 v[84:87], a[104:107], v[124:127], v[84:87]\n"
    "s_waitcnt lgkmcnt(6)\n"
    "buffer_load_dwordx4 v[24:27], v165, s[52:55], s43 offen offset:0 sc1\n"
    "v_fmac_f32 v93, v112, v153\n"
    "v_mfma_f32_16x16x32_bf16 v[64:67], a[108:111], v[128:131], v[64:67]\n"
    "ds_read_b128 v[120:123], v167 offset:22528\n"
    "v_fmac_f32 v94, v112, v154\n"
    "v_fmac_f32 v95, v112, v155\n"
    "v_mfma_f32_16x16x32_bf16 v[68:71], a[112:115], v[128:131], v[68:71]\n"
    "ds_read_b128 v[124:127], v169 offset:22528\n"
    "v_mul_f32 v8, v92, v92\n"
    "v_mul_f32 v9, v93, v93\n"
    "v_mfma_f32_16x16x32_bf16 v[72:75], a[116:119], v[128:131], v[72:75]\n"
    "s_waitcnt lgkmcnt(7)\n"
    "s_waitcnt vmcnt(13)\n"
    "ds_write2st64_b64 v164, v[56:57], v[58:59] offset0:64 offset1:66\n"
    "v_mfma_f32_16x16x32_bf16 v[76:79], a[108:111], v[132:135], v[76:79]\n"
    "buffer_load_dwordx4 v[28:31], v165, s[52:55], s43 offen offset:1024 sc1\n"
    "v_mul_f32 v10, v94, v94\n"
    "v_mfma_f32_16x16x32_bf16 v[80:83], a[112:115], v[132:135], v[80:83]\n"
    "v_mul_f32 v11, v95, v95\n"
    "v_fma_f32 v8, v8, v114, v113\n"
    "v_fma_f32 v9, v9, v114, v113\n"
    "v_mfma_f32_16x16x32_bf16 v[84:87], a[116:119], v[132:135], v[84:87]\n"
    "s_waitcnt lgkmcnt(6)\n"
    "v_fma_f32 v10, v10, v114, v113\n"
    "v_fma_f32 v11, v11, v114, v113\n"
    "v_mul_f32 v8, v8, v92\n"
    "v_mfma_f32_16x16x32_bf16 v[16:19], v[136:139], v[136:139], v[16:19]\n"
    "ds_read_b128 v[128:131], v166 offset:24576\n"
    "v_mul_f32 v9, v9, v93\n"
    "v_mul_f32 v10, v10, v94\n"
    "v_mfma_f32_16x16x32_bf16 v[20:23], v[144:147], v[136:139], v[20:23]\n"
    "ds_read_b128 v[132:135], v168 offset:24576\n"
    "v_mul_f32 v11, v11, v95\n"
    "v_fmac_f32 v100, v112, v188\n"
    "v_mfma_f32_16x16x32_bf16 v[64:67], a[120:123], v[136:139], v[64:67]\n"
    "buffer_load_dwordx4 v[32:35], v165, s[52:55], s43 offen offset:2048 sc1\n"
    "v_fmac_f32 v101, v112, v189\n"
    "v_mfma_f32_16x16x32_bf16 v[68:71], a[124:127], v[136:139], v[68:71]\n"
    "s_waitcnt vmcnt(14)\n"
    "ds_write2st64_b64 v164, v[60:61], v[62:63] offset0:72 offset1:74\n"
    "v_mfma_f32_16x16x32_bf16 v[72:75], a[128:131], v[136:139], v[72:75]\n"
    "s_waitcnt lgkmcnt(8)\n"
    "v_exp_f32 v8, v8\n"
    "v_fmac_f32 v102, v112, v190\n"
    "v_mfma_f32_16x16x32_bf16 v[76:79], a[120:123], v[140:143], v[76:79]\n"
    "buffer_load_dwordx4 v[36:39], v165, s[52:55], s43 offen offset:3072 sc1\n"
    "v_fmac_f32 v103, v112, v191\n"
    "v_mfma_f32_16x16x32_bf16 v[80:83], a[124:127], v[140:143], v[80:83]\n"
    "s_add_u32 s43, s39, 23552\n"
    "v_exp_f32 v9, v9\n"
    "v_fma_f32 v8, v8, v115, v115\n"
    "v_mfma_f32_16x16x32_bf16 v[84:87], a[128:131], v[140:143], v[84:87]\n"
    "s_waitcnt lgkmcnt(5)\n"
    "v_exp_f32 v10, v10\n"
    "v_fma_f32 v9, v9, v115, v115\n"
    "v_mfma_f32_16x16x32_bf16 v[64:67], a[132:135], v[120:123], v[64:67]\n"
    "ds_read_b128 v[136:139], v167 offset:26624\n"
    "v_exp_f32 v11, v11\n"
    "v_mfma_f32_16x16x32_bf16 v[68:71], a[136:139], v[120:123], v[68:71]\n"
    "ds_read_b128 v[140:143], v169 offset:26624\n"
    "v_fma_f32 v10, v10, v115, v115\n"
    "v_fma_f32 v11, v11, v115, v115\n"
    "v_mfma_f32_16x16x32_bf16 v[72:75], a[140:143], v[120:123], v[72:75]\n"
    "s_waitcnt lgkmcnt(6)\n"
    "buffer_load_dwordx4 v[40:43], v165, s[52:55], s43 offen offset:0 sc1\n"
    "v_mul_f32 v92, v92, v100\n"
    "v_mfma_f32_16x16x32_bf16 v[76:79], a[132:135], v[124:127], v[76:79]\n"
    "v_rcp_f32 v8, v8\n"
    "v_mul_f32 v93, v93, v101\n"
    "v_mfma_f32_16x16x32_bf16 v[80:83], a[136:139], v[124:127], v[80:83]\n"
    "v_rcp_f32 v9, v9\n"
    "v_mul_f32 v94, v94, v102\n"
    "v_mfma_f32_16x16x32_bf16 v[84:87], a[140:143], v[124:127], v[84:87]\n"
    "s_waitcnt lgkmcnt(4)\n"
    "buffer_load_dwordx4 v[44:47], v165, s[52:55], s43 offen offset:1024 sc1\n"
    "v_mul_f32 v95, v95, v103\n"
    "v_mfma_f32_16x16x32_bf16 v[16:19], v[128:131], v[128:131], v[16:19]\n"
    "ds_read_b128 v[120:123], v166 offset:28672\n"
    "v_mul_f32 v92, v92, v8\n"
    "v_mfma_f32_16x16x32_bf16 v[20:23], v[144:147], v[128:131], v[20:23]\n"
    "ds_read_b128 v[124:127], v168 offset:28672\n"
    "v_mul_f32 v93, v93, v9\n"
    "v_mfma_f32_16x16x32_bf16 v[64:67], a[144:147], v[128:131], v[64:67]\n"
    "v_rcp_f32 v10, v10\n"
    "v_mfma_f32_16x16x32_bf16 v[68:71], a[148:151], v[128:131], v[68:71]\n"
    "buffer_load_dwordx4 v[48:51], v165, s[52:55], s43 offen offset:2048 sc1\n"
    "v_mfma_f32_16x16x32_bf16 v[72:75], a[152:155], v[128:131], v[72:75]\n"
    "s_waitcnt lgkmcnt(5)\n"
    "v_rcp_f32 v11, v11\n"
    "v_mfma_f32_16x16x32_bf16 v[76:79], a[144:147], v[132:135], v[76:79]\n"
    "v_mul_f32 v94, v94, v10\n"
    "v_mul_f32 v95, v95, v11\n"
    "v_mfma_f32_16x16x32_bf16 v[80:83], a[148:151], v[132:135], v[80:83]\n"
    "v_cvt_pk_bf16_f32 v4, v92, v93\n"
    "v_cvt_pk_bf16_f32 v5, v94, v95\n"
    "v_mfma_f32_16x16x32_bf16 v[84:87], a[152:155], v[132:135], v[84:87]\n"
    "s_waitcnt lgkmcnt(3)\n"
    "buffer_load_dwordx4 v[52:55], v165, s[52:55], s43 offen offset:3072 sc1\n"
    "v_mfma_f32_16x16x32_bf16 v[64:67], a[156:159], v[136:139], v[64:67]\n"
    "ds_read_b128 v[128:131], v167 offset:30720\n"
    "s_add_u32 s43, s39, 27648\n"
    "v_mfma_f32_16x16x32_bf16 v[68:71], a[160:163], v[136:139], v[68:71]\n"
    "ds_read_b128 v[132:135], v169 offset:30720\n"
    "v_mfma_f32_16x16x32_bf16 v[72:75], a[164:167], v[136:139], v[72:75]\n"
    "s_waitcnt lgkmcnt(4)\n"
    "buffer_store_dwordx4 v[2:5], v176, s[52:55], s9 offen sc1\n"
    "v_mfma_f32_16x16x32_bf16 v[76:79], a[156:159], v[140:143], v[76:79]\n"
    "v_fmac_f32 v96, v112, v156\n"
    "v_fmac_f32 v97, v112, v157\n"
    "v_mfma_f32_16x16x32_bf16 v[80:83], a[160:163], v[140:143], v[80:83]\n"
    "v_fmac_f32 v98, v112, v158\n"
    "v_fmac_f32 v99, v112, v159\n"
    "v_mfma_f32_16x16x32_bf16 v[84:87], a[164:167], v[140:143], v[84:87]\n"
    "s_waitcnt lgkmcnt(3)\n"
    "v_mul_f32 v8, v96, v96\n"
    "v_mul_f32 v9, v97, v97\n"
    "v_mfma_f32_16x16x32_bf16 v[16:19], v[120:123], v[120:123], v[16:19]\n"
    "ds_read_b128 v[136:139], v166 offset:32768\n"
    "v_mul_f32 v10, v98, v98\n"
    "v_mfma_f32_16x16x32_bf16 v[20:23], v[144:147], v[120:123], v[20:23]\n"
    "ds_read_b128 v[140:143], v168 offset:32768\n"
    "v_mul_f32 v11, v99, v99\n"
    "v_mfma_f32_16x16x32_bf16 v[64:67], a[168:171], v[120:123], v[64:67]\n"
    "buffer_load_dwordx4 v[56:59], v165, s[52:55], s43 offen offset:0 sc1\n"
    "v_mfma_f32_16x16x32_bf16 v[68:71], a[172:175], v[120:123], v[68:71]\n"
    "v_fma_f32 v8, v8, v114, v113\n"
    "v_fma_f32 v9, v9, v114, v113\n"
    "v_mfma_f32_16x16x32_bf16 v[72:75], a[176:179], v[120:123], v[72:75]\n"
    "s_waitcnt lgkmcnt(4)\n"
    "v_fma_f32 v10, v10, v114, v113\n"
    "v_fma_f32 v11, v11, v114, v113\n"
    "v_mfma_f32_16x16x32_bf16 v[76:79], a[168:171], v[124:127], v[76:79]\n"
    "v_mul_f32 v8, v8, v96\n"
    "v_mul_f32 v9, v9, v97\n"
    "v_mfma_f32_16x16x32_bf16 v[80:83], a[172:175], v[124:127], v[80:83]\n"
    "buffer_load_dwordx4 v[60:63], v165, s[52:55], s43 offen offset:1024 sc1\n"
    "v_mfma_f32_16x16x32_bf16 v[84:87], a[176:179], v[124:127], v[84:87]\n"
    "s_waitcnt lgkmcnt(3)\n"
    "v_mul_f32 v10, v10, v98\n"
    "v_mul_f32 v11, v11, v99\n"
    "v_mfma_f32_16x16x32_bf16 v[64:67], a[180:183], v[128:131], v[64:67]\n"
    "ds_read_b128 v[120:123], v167 offset:34816\n"
    "v_fmac_f32 v108, v112, v192\n"
    "v_mfma_f32_16x16x32_bf16 v[68:71], a[184:187], v[128:131], v[68:71]\n"
    "ds_read_b128 v[124:127], v169 offset:34816\n"
    "v_fmac_f32 v109, v112, v193\n"
    "v_mfma_f32_16x16x32_bf16 v[72:75], a[188:191], v[128:131], v[72:75]\n"
    "s_waitcnt lgkmcnt(4)\n"
    "v_exp_f32 v8, v8\n"
    "v_mfma_f32_16x16x32_bf16 v[76:79], a[180:183], v[132:135], v[76:79]\n"
    "v_exp_f32 v9, v9\n"
    "v_mfma_f32_16x16x32_bf16 v[80:83], a[184:187], v[132:135], v[80:83]\n"
    "v_exp_f32 v10, v10\n"
    "v_mfma_f32_16x16x32_bf16 v[84:87], a[188:191], v[132:135], v[84:87]\n"
    "s_waitcnt lgkmcnt(3)\n"
    "v_exp_f32 v11, v11\n"
    "v_mfma_f32_16x16x32_bf16 v[16:19], v[136:139], v[136:139], v[16:19]\n"
    "ds_read_b128 v[128:131], v166 offset:36864\n"
    "v_fmac_f32 v110, v112, v194\n"
    "v_mfma_f32_16x16x32_bf16 v[20:23], v[144:147], v[136:139], v[20:23]\n"
    "ds_read_b128 v[132:135], v168 offset:36864\n"
    "v_fmac_f32 v111, v112, v195\n"
    "v_mfma_f32_16x16x32_bf16 v[64:67], a[192:195], v[136:139], v[64:67]\n"
    "v_fma_f32 v8, v8, v115, v115\n"
    "v_fma_f32 v9, v9, v115, v115\n"
    "v_mfma_f32_16x16x32_bf16 v[68:71], a[196:199], v[136:139], v[68:71]\n"
    "v_fma_f32 v10, v10, v115, v115\n"
    "v_fma_f32 v11, v11, v115, v115\n"
    "v_mfma_f32_16x16x32_bf16 v[72:75], a[200:203], v[136:139], v[72:75]\n"
    "s_waitcnt lgkmcnt(4)\n"
    "v_rcp_f32 v8, v8\n"
    "v_mfma_f32_16x16x32_bf16 v[76:79], a[192:195], v[140:143], v[76:79]\n"
    "v_rcp_f32 v9, v9\n"
    "v_mfma_f32_16x16x32_bf16 v[80:83], a[196:199], v[140:143], v[80:83]\n"
    "v_rcp_f32 v10, v10\n"
    "v_mfma_f32_16x16x32_bf16 v[84:87], a[200:203], v[140:143], v[84:87]\n"
    "s_waitcnt lgkmcnt(3)\n"
    "v_rcp_f32 v11, v11\n"
    "v_mfma_f32_16x16x32_bf16 v[64:67], a[204:207], v[120:123], v[64:67]\n"
    "ds_read_b128 v[136:139], v167 offset:38912\n"
    "v_mul_f32 v96, v96, v108\n"
    "v_mfma_f32_16x16x32_bf16 v[68:71], a[208:211], v[120:123], v[68:71]\n"
    "ds_read_b128 v[140:143], v169 offset:38912\n"
    "v_mul_f32 v97, v97, v109\n"
    "v_mfma_f32_16x16x32_bf16 v[72:75], a[212:215], v[120:123], v[72:75]\n"
    "s_waitcnt lgkmcnt(4)\n"
    "v_mul_f32 v98, v98, v110\n"
    "v_mul_f32 v99, v99, v111\n"
    "v_mfma_f32_16x16x32_bf16 v[76:79], a[204:207], v[124:127], v[76:79]\n"
    "v_mul_f32 v96, v96, v8\n"
    "v_mul_f32 v97, v97, v9\n"
    "v_mfma_f32_16x16x32_bf16 v[80:83], a[208:211], v[124:127], v[80:83]\n"
    "v_mul_f32 v98, v98, v10\n"
    "v_mul_f32 v99, v99, v11\n"
    "v_mfma_f32_16x16x32_bf16 v[84:87], a[212:215], v[124:127], v[84:87]\n"
    "s_waitcnt lgkmcnt(3)\n"
    "v_cvt_pk_bf16_f32 v6, v96, v97\n"
    "v_cvt_pk_bf16_f32 v7, v98, v99\n"
    "v_mfma_f32_16x16x32_bf16 v[16:19], v[128:131], v[128:131], v[16:19]\n"
    "buffer_store_dwordx2 v[6:7], v177, s[52:55], s9 offen offset:2048 sc1\n"
    "v_mfma_f32_16x16x32_bf16 v[20:23], v[144:147], v[128:131], v[20:23]\n"
    "v_mfma_f32_16x16x32_bf16 v[64:67], a[216:219], v[128:131], v[64:67]\n"
    "v_mfma_f32_16x16x32_bf16 v[68:71], a[220:223], v[128:131], v[68:71]\n"
    "v_mfma_f32_16x16x32_bf16 v[72:75], a[224:227], v[128:131], v[72:75]\n"
    "s_waitcnt lgkmcnt(2)\n"
    "v_mfma_f32_16x16x32_bf16 v[76:79], a[216:219], v[132:135], v[76:79]\n"
    "s_nop 2\n"
    "v_cndmask_b32 v21, v16, v17, s[32:33]\n"
    "v_cndmask_b32 v21, v21, v18, s[34:35]\n"
    "v_mfma_f32_16x16x32_bf16 v[80:83], a[220:223], v[132:135], v[80:83]\n"
    "v_cndmask_b32 v21, v21, v19, s[36:37]\n"
    "ds_write_b64 v174, v[20:21] offset:0\n"
    "v_mfma_f32_16x16x32_bf16 v[84:87], a[224:227], v[132:135], v[84:87]\n"
    "s_waitcnt lgkmcnt(2)\n"
    "s_waitcnt lgkmcnt(0)\n"
    "s_barrier\n"
    "ds_read_b128 v[120:123], v170 offset:0\n"
    "ds_read_b128 v[124:127], v172 offset:0\n"
    "v_mfma_f32_16x16x32_bf16 v[64:67], a[228:231], v[136:139], v[64:67]\n"
    "ds_read_b128 v[128:131], v171 offset:2048\n"
    "v_mfma_f32_16x16x32_bf16 v[68:71], a[232:235], v[136:139], v[68:71]\n"
    "ds_read_b128 v[132:135], v173 offset:2048\n"
    "v_mfma_f32_16x16x32_bf16 v[72:75], a[236:239], v[136:139], v[72:75]\n"
    "ds_read_b128 v[8:11], v175 offset:0\n"
    "v_mfma_f32_16x16x32_bf16 v[76:79], a[228:231], v[140:143], v[76:79]\n"
    "v_mfma_f32_16x16x32_bf16 v[80:83], a[232:235], v[140:143], v[80:83]\n"
    "v_mfma_f32_16x16x32_bf16 v[84:87], a[236:239], v[140:143], v[84:87]\n"
    "s_waitcnt lgkmcnt(4)\n"
    "v_mfma_f32_16x16x32_bf16 v[16:19], v[120:123], v[120:123], 0\n"
    "ds_read_b128 v[136:139], v170 offset:4096\n"
    "v_mfma_f32_16x16x32_bf16 v[20:23], v[144:147], v[120:123], 0\n"
    "ds_read_b128 v[140:143], v172 offset:4096\n"
    "v_mfma_f32_16x16x32_bf16 v[88:91], a[0:3], v[120:123], 0\n"
    "s_waitcnt lgkmcnt(2)\n"
    "v_add_f32 v8, v8, v10\n"
    "v_add_f32 v9, v9, v11\n"
    "v_mul_f32 v8, 0x3acccccd, v8\n"
    "v_mfma_f32_16x16x32_bf16 v[92:95], a[4:7], v[120:123], 0\n"
    "v_mul_f32 v9, 0x3acccccd, v9\n"
    "v_fma_f32 v9, -v8, v8, v9\n"
    "v_max_f32 v9, 0, v9\n"
    "v_mfma_f32_16x16x32_bf16 v[96:99], a[8:11], v[120:123], 0\n"
    "v_cvt_pk_bf16_f32 v8, -v8, v9\n"
    "v_and_b32 v9, 0xffff0000, v8\n"
    "v_lshlrev_b32 v12, 16, v8\n"
    "v_mfma_f32_16x16x32_bf16 v[100:103], a[0:3], v[124:127], 0\n"
    "v_add_f32 v9, 0x3727c5ac, v9\n"
    "v_cvt_pk_bf16_f32 v9, v9, v9\n"
    "v_and_b32 v9, 0xffff0000, v9\n"
    "v_mfma_f32_16x16x32_bf16 v[104:107], a[4:7], v[124:127], 0\n"
    "v_sqrt_f32 v9, v9\n"
    "s_nop 0\n"
    "v_cvt_pk_bf16_f32 v9, v9, v9\n"
    "v_mfma_f32_16x16x32_bf16 v[108:111], a[8:11], v[124:127], 0\n"
    "s_waitcnt vmcnt(0)\n"
    "s_barrier\n"
    "v_mov_b32 v116, s64\n"
    "v_mov_b32 v117, s65\n"
    "v_cvt_f32_u32 v118, v0\n"
    "buffer_store_dwordx2 v[116:117], v118, s[52:55], s5 offen offset:0 sc1\n"
    "v_mfma_f32_16x16x32_bf16 v[88:91], a[12:15], v[128:131], v[88:91]\n"
    "ds_read_b128 v[120:123], v171 offset:6144\n"
    "v_lshrrev_b32 v168, 4, v0\n"
    "v_and_b32 v167, 15, v0\n"
    "v_mfma_f32_16x16x32_bf16 v[92:95], a[16:19], v[128:131], v[92:95]\n"
    "ds_read_b128 v[124:127], v173 offset:6144\n"
    "v_lshlrev_b32 v166, 9, v168\n"
    "v_lshl_add_u32 v166, v167, 4, v166\n"
    "v_mfma_f32_16x16x32_bf16 v[96:99], a[20:23], v[128:131], v[96:99]\n"
    "s_add_u32 s43, s39, 29696\n"
    "v_mul_u32_u24 v167, 80, v167\n"
    "v_add_lshl_u32 v168, v168, v167, 4\n"
    "v_bfrev_b32 v169, 1\n"
    "v_mfma_f32_16x16x32_bf16 v[100:103], a[12:15], v[132:135], v[100:103]\n"
    "buffer_load_dwordx4 a[0:3], v165, s[52:55], s43 offen offset:0 sc1\n"
    "v_and_b32 v9, 0xffff0000, v9\n"
    "v_mfma_f32_16x16x32_bf16 v[104:107], a[16:19], v[132:135], v[104:107]\n"
    "s_add_u32 s10, s40, 81920\n"
    "v_cmp_gt_u32 vcc, 160, v0\n"
    "s_nop 1\n"
    "v_cndmask_b32 v167, v169, v166, vcc\n"
    "v_cndmask_b32 v168, v169, v168, vcc\n"
    "v_mfma_f32_16x16x32_bf16 v[108:111], a[20:23], v[132:135], v[108:111]\n"
    "s_waitcnt lgkmcnt(3)\n"
    "v_add_u32 v169, s10, v165\n"
    "v_add_u32 v166, 81920, v166\n"
    "v_mul_f32 v15, v9, v9\n"
    "v_mfma_f32_16x16x32_bf16 v[16:19], v[136:139], v[136:139], v[16:19]\n"
    "ds_read_b128 v[128:131], v170 offset:8192\n"
    "v_rcp_f32 v10, v9\n"
    "v_mfma_f32_16x16x32_bf16 v[20:23], v[144:147], v[136:139], v[20:23]\n"
    "ds_read_b128 v[132:135], v172 offset:8192\n"
    "v_mul_f32 v11, v10, v10\n"
    "v_mul_f32 v13, 0xc0135761, v10\n"
    "v_mfma_f32_16x16x32_bf16 v[88:91], a[24:27], v[136:139], v[88:91]\n"
    "buffer_load_dwordx4 a[4:7], v165, s[52:55], s43 offen offset:1024 sc1\n"
    "v_mul_f32 v11, 0x3d372713, v11\n"
    "v_mfma_f32_16x16x32_bf16 v[92:95], a[28:31], v[136:139], v[92:95]\n"
    "v_mul_f32 v14, v11, v13\n"
    "v_permlane32_swap_b32 v64, v76\n"
    "v_mfma_f32_16x16x32_bf16 v[96:99], a[32:35], v[136:139], v[96:99]\n"
    "s_waitcnt lgkmcnt(4)\n"
    "v_permlane32_swap_b32 v65, v77\n"
    "v_mfma_f32_16x16x32_bf16 v[100:103], a[24:27], v[140:143], v[100:103]\n"
    "buffer_load_dwordx4 a[8:11], v165, s[52:55], s43 offen offset:2048 sc1\n"
    "v_mfma_f32_16x16x32_bf16 v[104:107], a[28:31], v[140:143], v[104:107]\n"
    "v_permlane32_swap_b32 v66, v78\n"
    "v_mfma_f32_16x16x32_bf16 v[108:111], a[32:35], v[140:143], v[108:111]\n"
    "s_waitcnt lgkmcnt(3)\n"
    "v_permlane32_swap_b32 v67, v79\n"
    "v_fmac_f32 v64, v12, v148\n"
    "v_mfma_f32_16x16x32_bf16 v[88:91], a[36:39], v[120:123], v[88:91]\n"
    "ds_read_b128 v[136:139], v171 offset:10240\n"
    "v_permlane32_swap_b32 v68, v80\n"
    "v_mfma_f32_16x16x32_bf16 v[92:95], a[40:43], v[120:123], v[92:95]\n"
    "ds_read_b128 v[140:143], v173 offset:10240\n"
    "v_permlane32_swap_b32 v69, v81\n"
    "v_mfma_f32_16x16x32_bf16 v[96:99], a[44:47], v[120:123], v[96:99]\n"
    "s_waitcnt lgkmcnt(4)\n"
    "buffer_load_dwordx4 a[12:15], v165, s[52:55], s43 offen offset:3072 sc1\n"
    "v_fmac_f32 v65, v12, v149\n"
    "v_mfma_f32_16x16x32_bf16 v[100:103], a[36:39], v[124:127], v[100:103]\n"
    "s_add_u32 s43, s39, 33792\n"
    "v_permlane32_swap_b32 v70, v82\n"
    "v_fmac_f32 v66, v12, v150\n"
    "v_mfma_f32_16x16x32_bf16 v[104:107], a[40:43], v[124:127], v[104:107]\n"
    "v_permlane32_swap_b32 v71, v83\n"
    "v_fmac_f32 v67, v12, v151\n"
    "v_mfma_f32_16x16x32_bf16 v[108:111], a[44:47], v[124:127], v[108:111]\n"
    "s_waitcnt lgkmcnt(3)\n"
    "buffer_load_dwordx4 a[16:19], v165, s[52:55], s43 offen offset:0 sc1\n"
    "v_mul_f32 v8, v64, v64\n"
    "v_mfma_f32_16x16x32_bf16 v[16:19], v[128:131], v[128:131], v[16:19]\n"
    "ds_read_b128 v[120:123], v170 offset:12288\n"
    "v_permlane32_swap_b32 v72, v84\n"
    "v_mfma_f32_16x16x32_bf16 v[20:23], v[144:147], v[128:131], v[20:23]\n"
    "ds_read_b128 v[124:127], v172 offset:12288\n"
    "v_permlane32_swap_b32 v73, v85\n"
    "v_mfma_f32_16x16x32_bf16 v[88:91], a[48:51], v[128:131], v[88:91]\n"
    "buffer_load_dwordx4 a[20:23], v165, s[52:55], s43 offen offset:1024 sc1\n"
    "v_mul_f32 v9, v65, v65\n"
    "v_mfma_f32_16x16x32_bf16 v[92:95], a[52:55], v[128:131], v[92:95]\n"
    "v_permlane32_swap_b32 v74, v86\n"
    "v_mul_f32 v10, v66, v66\n"
    "v_mfma_f32_16x16x32_bf16 v[96:99], a[56:59], v[128:131], v[96:99]\n"
    "s_waitcnt lgkmcnt(4)\n"
    "v_permlane32_swap_b32 v75, v87\n"
    "v_mul_f32 v11, v67, v67\n"
    "v_mfma_f32_16x16x32_bf16 v[100:103], a[48:51], v[132:135], v[100:103]\n"
    "buffer_load_dwordx4 a[24:27], v165, s[52:55], s43 offen offset:2048 sc1\n"
    "v_fma_f32 v8, v8, v14, v13\n"
    "v_mfma_f32_16x16x32_bf16 v[104:107], a[52:55], v[132:135], v[104:107]\n"
    "v_fma_f32 v9, v9, v14, v13\n"
    "v_fma_f32 v10, v10, v14, v13\n"
    "v_fma_f32 v11, v11, v14, v13\n"
    "v_mfma_f32_16x16x32_bf16 v[108:111], a[56:59], v[132:135], v[108:111]\n"
    "s_waitcnt lgkmcnt(3)\n"
    "v_mul_f32 v8, v8, v64\n"
    "v_mul_f32 v9, v9, v65\n"
    "v_mul_f32 v10, v10, v66\n"
    "v_mfma_f32_16x16x32_bf16 v[88:91], a[60:63], v[136:139], v[88:91]\n"
    "ds_read_b128 v[128:131], v171 offset:14336\n"
    "v_mul_f32 v11, v11, v67\n"
    "v_fmac_f32 v80, v12, v184\n"
    "v_mfma_f32_16x16x32_bf16 v[92:95], a[64:67], v[136:139], v[92:95]\n"
    "ds_read_b128 v[132:135], v173 offset:14336\n"
    "v_exp_f32 v8, v8\n"
    "v_mfma_f32_16x16x32_bf16 v[96:99], a[68:71], v[136:139], v[96:99]\n"
    "s_waitcnt lgkmcnt(4)\n"
    "buffer_load_dwordx4 a[28:31], v165, s[52:55], s43 offen offset:3072 sc1\n"
    "v_fmac_f32 v81, v12, v185\n"
    "v_mfma_f32_16x16x32_bf16 v[100:103], a[60:63], v[140:143], v[100:103]\n"
    "s_add_u32 s43, s39, 37888\n"
    "v_exp_f32 v9, v9\n"
    "v_fmac_f32 v82, v12, v186\n"
    "v_mfma_f32_16x16x32_bf16 v[104:107], a[64:67], v[140:143], v[104:107]\n"
    "v_exp_f32 v10, v10\n"
    "v_fmac_f32 v83, v12, v187\n"
    "v_mfma_f32_16x16x32_bf16 v[108:111], a[68:71], v[140:143], v[108:111]\n"
    "s_waitcnt lgkmcnt(3)\n"
    "buffer_load_dwordx4 a[32:35], v165, s[52:55], s43 offen offset:0 sc1\n"
    "v_fma_f32 v8, v8, v15, v15\n"
    "v_mfma_f32_16x16x32_bf16 v[16:19], v[120:123], v[120:123], v[16:19]\n"
    "ds_read_b128 v[136:139], v170 offset:16384\n"
    "v_exp_f32 v11, v11\n"
    "v_mfma_f32_16x16x32_bf16 v[20:23], v[144:147], v[120:123], v[20:23]\n"
    "ds_read_b128 v[140:143], v172 offset:16384\n"
    "v_fma_f32 v9, v9, v15, v15\n"
    "v_mfma_f32_16x16x32_bf16 v[88:91], a[72:75], v[120:123], v[88:91]\n"
    "buffer_load_dwordx4 a[36:39], v165, s[52:55], s43 offen offset:1024 sc1\n"
    "v_mfma_f32_16x16x32_bf16 v[92:95], a[76:79], v[120:123], v[92:95]\n"
    "v_fma_f32 v10, v10, v15, v15\n"
    "v_fma_f32 v11, v11, v15, v15\n"
    "v_mfma_f32_16x16x32_bf16 v[96:99], a[80:83], v[120:123], v[96:99]\n"
    "s_waitcnt lgkmcnt(4)\n"
    "v_rcp_f32 v8, v8\n"
    "v_mfma_f32_16x16x32_bf16 v[100:103], a[72:75], v[124:127], v[100:103]\n"
    "buffer_load_dwordx4 a[40:43], v165, s[52:55], s43 offen offset:2048 sc1\n"
    "v_mfma_f32_16x16x32_bf16 v[104:107], a[76:79], v[124:127], v[104:107]\n"
    "v_rcp_f32 v9, v9\n"
    "v_mfma_f32_16x16x32_bf16 v[108:111], a[80:83], v[124:127], v[108:111]\n"
    "s_waitcnt lgkmcnt(3)\n"
    "v_rcp_f32 v10, v10\n"
    "v_mfma_f32_16x16x32_bf16 v[88:91], a[84:87], v[128:131], v[88:91]\n"
    "ds_read_b128 v[120:123], v171 offset:18432\n"
    "v_mul_f32 v64, v64, v80\n"
    "v_mfma_f32_16x16x32_bf16 v[92:95], a[88:91], v[128:131], v[92:95]\n"
    "ds_read_b128 v[124:127], v173 offset:18432\n"
    "v_mul_f32 v65, v65, v81\n"
    "v_mfma_f32_16x16x32_bf16 v[96:99], a[92:95], v[128:131], v[96:99]\n"
    "s_waitcnt lgkmcnt(4)\n"
    "buffer_load_dwordx2 v[160:161], v163, s[52:55], 0 offen sc1\n"
    "v_mfma_f32_16x16x32_bf16 v[100:103], a[84:87], v[132:135], v[100:103]\n"
    "s_add_u32 s43, s39, 40960\n"
    "v_rcp_f32 v11, v11\n"
    "v_mfma_f32_16x16x32_bf16 v[104:107], a[88:91], v[132:135], v[104:107]\n"
    "v_mul_f32 v66, v66, v82\n"
    "v_mul_f32 v67, v67, v83\n"
    "v_mfma_f32_16x16x32_bf16 v[108:111], a[92:95], v[132:135], v[108:111]\n"
    "s_waitcnt lgkmcnt(3)\n"
    "buffer_load_dwordx4 a[44:47], v165, s[52:55], s43 offen offset:0 sc1\n"
    "v_mfma_f32_16x16x32_bf16 v[16:19], v[136:139], v[136:139], v[16:19]\n"
    "ds_read_b128 v[128:131], v170 offset:20480\n"
    "v_mul_f32 v64, v64, v8\n"
    "v_mfma_f32_16x16x32_bf16 v[20:23], v[144:147], v[136:139], v[20:23]\n"
    "ds_read_b128 v[132:135], v172 offset:20480\n"
    "v_mul_f32 v65, v65, v9\n"
    "v_mfma_f32_16x16x32_bf16 v[88:91], a[96:99], v[136:139], v[88:91]\n"
    "buffer_load_dwordx4 a[48:51], v165, s[52:55], s43 offen offset:1024 sc1\n"
    "v_mul_f32 v66, v66, v10\n"
    "v_mfma_f32_16x16x32_bf16 v[92:95], a[100:103], v[136:139], v[92:95]\n"
    "v_mul_f32 v67, v67, v11\n"
    "v_cvt_pk_bf16_f32 v2, v64, v65\n"
    "v_mfma_f32_16x16x32_bf16 v[96:99], a[104:107], v[136:139], v[96:99]\n"
    "s_waitcnt lgkmcnt(4)\n"
    "v_cvt_pk_bf16_f32 v3, v66, v67\n"
    "s_add_u32 s9, s38, 196608\n"
    "v_fmac_f32 v68, v12, v152\n"
    "v_mfma_f32_16x16x32_bf16 v[100:103], a[96:99], v[140:143], v[100:103]\n"
    "buffer_load_dwordx4 a[52:55], v165, s[52:55], s43 offen offset:2048 sc1\n"
    "v_fmac_f32 v69, v12, v153\n"
    "v_mfma_f32_16x16x32_bf16 v[104:107], a[100:103], v[140:143], v[104:107]\n"
    "v_fmac_f32 v70, v12, v154\n"
    "v_fmac_f32 v71, v12, v155\n"
    "v_mfma_f32_16x16x32_bf16 v[108:111], a[104:107], v[140:143], v[108:111]\n"
    "s_waitcnt lgkmcnt(3)\n"
    "v_mul_f32 v8, v68, v68\n"
    "v_mul_f32 v9, v69, v69\n"
    "v_mfma_f32_16x16x32_bf16 v[88:91], a[108:111], v[120:123], v[88:91]\n"
    "ds_read_b128 v[136:139], v171 offset:22528\n"
    "v_mul_f32 v10, v70, v70\n"
    "v_mfma_f32_16x16x32_bf16 v[92:95], a[112:115], v[120:123], v[92:95]\n"
    "ds_read_b128 v[140:143], v173 offset:22528\n"
    "v_mul_f32 v11, v71, v71\n"
    "v_mfma_f32_16x16x32_bf16 v[96:99], a[116:119], v[120:123], v[96:99]\n"
    "s_waitcnt lgkmcnt(4)\n"
    "buffer_load_dwordx4 a[56:59], v165, s[52:55], s43 offen offset:3072 sc1\n"
    "v_fma_f32 v8, v8, v14, v13\n"
    "v_mfma_f32_16x16x32_bf16 v[100:103], a[108:111], v[124:127], v[100:103]\n"
    "s_add_u32 s43, s39, 45056\n"
    "v_fma_f32 v9, v9, v14, v13\n"
    "v_fma_f32 v10, v10, v14, v13\n"
    "v_fma_f32 v11, v11, v14, v13\n"
    "v_mfma_f32_16x16x32_bf16 v[104:107], a[112:115], v[124:127], v[104:107]\n"
    "v_mul_f32 v8, v8, v68\n"
    "v_mul_f32 v9, v9, v69\n"
    "v_mfma_f32_16x16x32_bf16 v[108:111], a[116:119], v[124:127], v[108:111]\n"
    "s_waitcnt lgkmcnt(3)\n"
    "buffer_load_dwordx4 a[60:63], v165, s[52:55], s43 offen offset:0 sc1\n"
    "v_mul_f32 v10, v10, v70\n"
    "v_mfma_f32_16x16x32_bf16 v[16:19], v[128:131], v[128:131], v[16:19]\n"
    "ds_read_b128 v[120:123], v170 offset:24576\n"
    "v_mul_f32 v11, v11, v71\n"
    "v_mfma_f32_16x16x32_bf16 v[20:23], v[144:147], v[128:131], v[20:23]\n"
    "ds_read_b128 v[124:127], v172 offset:24576\n"
    "v_exp_f32 v8, v8\n"
    "v_mfma_f32_16x16x32_bf16 v[88:91], a[120:123], v[128:131], v[88:91]\n"
    "ds_read_b128 a[112:115], v178 offset:0\n"
    "v_exp_f32 v9, v9\n"
    "v_mfma_f32_16x16x32_bf16 v[92:95], a[124:127], v[128:131], v[92:95]\n"
    "ds_read_b128 a[116:119], v178 offset:4096\n"
    "v_exp_f32 v10, v10\n"
    "v_mfma_f32_16x16x32_bf16 v[96:99], a[128:131], v[128:131], v[96:99]\n"
    "s_waitcnt lgkmcnt(6)\n"
    "v_exp_f32 v11, v11\n"
    "v_mfma_f32_16x16x32_bf16 v[100:103], a[120:123], v[132:135], v[100:103]\n"
    "v_fmac_f32 v76, v12, v188\n"
    "v_fmac_f32 v77, v12, v189\n"
    "v_mfma_f32_16x16x32_bf16 v[104:107], a[124:127], v[132:135], v[104:107]\n"
    "v_fmac_f32 v78, v12, v190\n"
    "v_fmac_f32 v79, v12, v191\n"
    "v_mfma_f32_16x16x32_bf16 v[108:111], a[128:131], v[132:135], v[108:111]\n"
    "s_waitcnt lgkmcnt(5)\n"
    "v_fma_f32 v8, v8, v15, v15\n"
    "v_fma_f32 v9, v9, v15, v15\n"
    "v_fma_f32 v10, v10, v15, v15\n"
    "v_mfma_f32_16x16x32_bf16 v[88:91], a[132:135], v[136:139], v[88:91]\n"
    "ds_read_b128 v[128:131], v171 offset:26624\n"
    "v_fma_f32 v11, v11, v15, v15\n"
    "v_mul_f32 v68, v68, v76\n"
    "v_mfma_f32_16x16x32_bf16 v[92:95], a[136:139], v[136:139], v[92:95]\n"
    "ds_read_b128 v[132:135], v173 offset:26624\n"
    "v_rcp_f32 v8, v8\n"
    "v_mfma_f32_16x16x32_bf16 v[96:99], a[140:143], v[136:139], v[96:99]\n"
    "s_waitcnt lgkmcnt(6)\n"
    "s_waitcnt vmcnt(5)\n"
    "v_cmp_ne_u64 vcc, s[64:65], v[160:161]\n"
    "s_cmp_eq_u64 vcc, 0\n"
    "s_cbranch_scc1 .L36_%=\n"
    "s_mov_b32 s98, 100000\n"
    ".L38_%=:\n"
    "buffer_load_dwordx2 v[160:161], v163, s[52:55], 0 offen sc1\n"
    "s_waitcnt vmcnt(0)\n"
    "v_cmp_ne_u64 vcc, s[64:65], v[160:161]\n"
    "s_cmp_eq_u64 vcc, 0\n"
    "s_cbranch_scc1 .L36_%=\n"
    "s_sub_u32 s98, s98, 1\n"
    "s_cbranch_scc0 .L38_%=\n"
    ".L36_%=:\n"
    "v_mfma_f32_16x16x32_bf16 v[100:103], a[132:135], v[140:143], v[100:103]\n"
    "ds_read_b128 a[120:123], v178 offset:8192\n"
    "v_mul_f32 v69, v69, v77\n"
    "v_mfma_f32_16x16x32_bf16 v[104:107], a[136:139], v[140:143], v[104:107]\n"
    "ds_read_b128 a[124:127], v178 offset:12288\n"
    "v_mul_f32 v70, v70, v78\n"
    "v_mfma_f32_16x16x32_bf16 v[108:111], a[140:143], v[140:143], v[108:111]\n"
    "s_waitcnt lgkmcnt(7)\n"
    "ds_read_b128 a[128:131], v178 offset:16384\n"
    "v_mul_f32 v71, v71, v79\n"
    "v_mfma_f32_16x16x32_bf16 v[16:19], v[120:123], v[120:123], v[16:19]\n"
    "ds_read_b128 v[136:139], v170 offset:28672\n"
    "v_mul_f32 v68, v68, v8\n"
    "v_mfma_f32_16x16x32_bf16 v[20:23], v[144:147], v[120:123], v[20:23]\n"
    "ds_read_b128 v[140:143], v172 offset:28672\n"
    "s_add_u32 s44, s30, 0\n"
    "v_mfma_f32_16x16x32_bf16 v[88:91], a[144:147], v[120:123], v[88:91]\n"
    "buffer_load_dwordx4 a[64:67], v165, s[52:55], s44 offen offset:0 sc1\n"
    "v_mfma_f32_16x16x32_bf16 v[92:95], a[148:151], v[120:123], v[92:95]\n"
    "ds_read_b128 a[132:135], v178 offset:20480\n"
    "v_mfma_f32_16x16x32_bf16 v[96:99], a[152:155], v[120:123], v[96:99]\n"
    "s_waitcnt lgkmcnt(10)\n"
    "ds_read_b128 a[136:139], v178 offset:24576\n"
    "v_mfma_f32_16x16x32_bf16 v[100:103], a[144:147], v[124:127], v[100:103]\n"
    "buffer_load_dwordx4 a[68:71], v165, s[52:55], s44 offen offset:1024 sc1\n"
    "v_mfma_f32_16x16x32_bf16 v[104:107], a[148:151], v[124:127], v[104:107]\n"
    "ds_read_b128 a[140:143], v178 offset:28672\n"
    "v_rcp_f32 v9, v9\n"
    "v_mfma_f32_16x16x32_bf16 v[108:111], a[152:155], v[124:127], v[108:111]\n"
    "s_waitcnt lgkmcnt(9)\n"
    "v_rcp_f32 v10, v10\n"
    "v_mul_f32 v69, v69, v9\n"
    "v_mfma_f32_16x16x32_bf16 v[88:91], a[156:159], v[128:131], v[88:91]\n"
    "ds_read_b128 v[120:123], v171 offset:30720\n"
    "v_rcp_f32 v11, v11\n"
    "v_mfma_f32_16x16x32_bf16 v[92:95], a[160:163], v[128:131], v[92:95]\n"
    "ds_read_b128 v[124:127], v173 offset:30720\n"
    "v_mul_f32 v70, v70, v10\n"
    "v_mfma_f32_16x16x32_bf16 v[96:99], a[164:167], v[128:131], v[96:99]\n"
    "s_waitcnt lgkmcnt(10)\n"
    "buffer_load_dwordx4 a[72:75], v165, s[52:55], s44 offen offset:2048 sc1\n"
    "v_mul_f32 v71, v71, v11\n"
    "v_mfma_f32_16x16x32_bf16 v[100:103], a[156:159], v[132:135], v[100:103]\n"
    "ds_read_b128 a[144:147], v178 offset:32768\n"
    "v_cvt_pk_bf16_f32 v4, v68, v69\n"
    "v_cvt_pk_bf16_f32 v5, v70, v71\n"
    "v_mfma_f32_16x16x32_bf16 v[104:107], a[160:163], v[132:135], v[104:107]\n"
    "ds_read_b128 a[148:151], v178 offset:36864\n"
    "v_mfma_f32_16x16x32_bf16 v[108:111], a[164:167], v[132:135], v[108:111]\n"
    "s_waitcnt lgkmcnt(8)\n"
    "buffer_store_dwordx4 v[2:5], v176, s[52:55], s9 offen sc1\n"
    "v_mfma_f32_16x16x32_bf16 v[16:19], v[136:139], v[136:139], v[16:19]\n"
    "ds_read_b128 v[128:131], v170 offset:32768\n"
    "v_fmac_f32 v72, v12, v156\n"
    "v_mfma_f32_16x16x32_bf16 v[20:23], v[144:147], v[136:139], v[20:23]\n"
    "ds_read_b128 v[132:135], v172 offset:32768\n"
    "v_fmac_f32 v73, v12, v157\n"
    "v_mfma_f32_16x16x32_bf16 v[88:91], a[168:171], v[136:139], v[88:91]\n"
    "buffer_load_dwordx4 a[76:79], v165, s[52:55], s44 offen offset:3072 sc1\n"
    "v_fmac_f32 v74, v12, v158\n"
    "v_mfma_f32_16x16x32_bf16 v[92:95], a[172:175], v[136:139], v[92:95]\n"
    "ds_read_b128 a[152:155], v178 offset:40960\n"
    "v_fmac_f32 v75, v12, v159\n"
    "v_mul_f32 v8, v72, v72\n"
    "v_mfma_f32_16x16x32_bf16 v[96:99], a[176:179], v[136:139], v[96:99]\n"
    "s_waitcnt lgkmcnt(10)\n"
    "ds_read_b128 a[156:159], v178 offset:45056\n"
    "v_mul_f32 v9, v73, v73\n"
    "v_mfma_f32_16x16x32_bf16 v[100:103], a[168:171], v[140:143], v[100:103]\n"
    "ds_read_b128 a[160:163], v178 offset:49152\n"
    "v_mul_f32 v10, v74, v74\n"
    "v_mul_f32 v11, v75, v75\n"
    "v_mfma_f32_16x16x32_bf16 v[104:107], a[172:175], v[140:143], v[104:107]\n"
    "ds_read_b128 a[164:167], v178 offset:53248\n"
    "v_fma_f32 v8, v8, v14, v13\n"
    "v_fma_f32 v9, v9, v14, v13\n"
    "v_mfma_f32_16x16x32_bf16 v[108:111], a[176:179], v[140:143], v[108:111]\n"
    "s_waitcnt lgkmcnt(9)\n"
    "s_add_u32 s44, s30, 4096\n"
    "v_fma_f32 v10, v10, v14, v13\n"
    "v_fma_f32 v11, v11, v14, v13\n"
    "v_mfma_f32_16x16x32_bf16 v[88:91], a[180:183], v[120:123], v[88:91]\n"
    "ds_read_b128 v[136:139], v171 offset:34816\n"
    "v_mul_f32 v8, v8, v72\n"
    "v_mfma_f32_16x16x32_bf16 v[92:95], a[184:187], v[120:123], v[92:95]\n"
    "ds_read_b128 v[140:143], v173 offset:34816\n"
    "v_mul_f32 v9, v9, v73\n"
    "v_mfma_f32_16x16x32_bf16 v[96:99], a[188:191], v[120:123], v[96:99]\n"
    "s_waitcnt lgkmcnt(10)\n"
    "buffer_load_dwordx4 a[80:83], v165, s[52:55], s44 offen offset:0 sc1\n"
    "v_mul_f32 v10, v10, v74\n"
    "v_mfma_f32_16x16x32_bf16 v[100:103], a[180:183], v[124:127], v[100:103]\n"
    "ds_read_b128 a[168:171], v178 offset:57344\n"
    "v_mul_f32 v11, v11, v75\n"
    "v_fmac_f32 v84, v12, v192\n"
    "v_mfma_f32_16x16x32_bf16 v[104:107], a[184:187], v[124:127], v[104:107]\n"
    "v_exp_f32 v8, v8\n"
    "v_mfma_f32_16x16x32_bf16 v[108:111], a[188:191], v[124:127], v[108:111]\n"
    "s_waitcnt lgkmcnt(8)\n"
    "buffer_load_dwordx4 a[84:87], v165, s[52:55], s44 offen offset:1024 sc1\n"
    "v_fmac_f32 v85, v12, v193\n"
    "v_mfma_f32_16x16x32_bf16 v[16:19], v[128:131], v[128:131], v[16:19]\n"
    "ds_read_b128 v[120:123], v170 offset:36864\n"
    "v_exp_f32 v9, v9\n"
    "v_mfma_f32_16x16x32_bf16 v[20:23], v[144:147], v[128:131], v[20:23]\n"
    "ds_read_b128 v[124:127], v172 offset:36864\n"
    "v_exp_f32 v10, v10\n"
    "v_mfma_f32_16x16x32_bf16 v[88:91], a[192:195], v[128:131], v[88:91]\n"
    "buffer_load_dwordx4 a[88:91], v165, s[52:55], s44 offen offset:2048 sc1\n"
    "v_fmac_f32 v86, v12, v194\n"
    "v_mfma_f32_16x16x32_bf16 v[92:95], a[196:199], v[128:131], v[92:95]\n"
    "v_exp_f32 v11, v11\n"
    "v_mfma_f32_16x16x32_bf16 v[96:99], a[200:203], v[128:131], v[96:99]\n"
    "s_waitcnt lgkmcnt(9)\n"
    "v_fmac_f32 v87, v12, v195\n"
    "v_fma_f32 v8, v8, v15, v15\n"
    "v_mfma_f32_16x16x32_bf16 v[100:103], a[192:195], v[132:135], v[100:103]\n"
    "buffer_load_dwordx4 a[92:95], v165, s[52:55], s44 offen offset:3072 sc1\n"
    "v_fma_f32 v9, v9, v15, v15\n"
    "v_mfma_f32_16x16x32_bf16 v[104:107], a[196:199], v[132:135], v[104:107]\n"
    "s_add_u32 s44, s30, 8192\n"
    "v_fma_f32 v10, v10, v15, v15\n"
    "v_fma_f32 v11, v11, v15, v15\n"
    "v_mfma_f32_16x16x32_bf16 v[108:111], a[200:203], v[132:135], v[108:111]\n"
    "s_waitcnt lgkmcnt(4)\n"
    "v_rcp_f32 v8, v8\n"
    "v_mfma_f32_16x16x32_bf16 v[88:91], a[204:207], v[136:139], v[88:91]\n"
    "ds_read_b128 v[128:131], v171 offset:38912\n"
    "v_rcp_f32 v9, v9\n"
    "v_mfma_f32_16x16x32_bf16 v[92:95], a[208:211], v[136:139], v[92:95]\n"
    "ds_read_b128 v[132:135], v173 offset:38912\n"
    "v_mul_f32 v72, v72, v84\n"
    "v_mfma_f32_16x16x32_bf16 v[96:99], a[212:215], v[136:139], v[96:99]\n"
    "s_waitcnt lgkmcnt(5)\n"
    "buffer_load_dwordx4 a[96:99], v165, s[52:55], s44 offen offset:0 sc1\n"
    "v_mul_f32 v73, v73, v85\n"
    "v_mfma_f32_16x16x32_bf16 v[100:103], a[204:207], v[140:143], v[100:103]\n"
    "v_rcp_f32 v10, v10\n"
    "v_mfma_f32_16x16x32_bf16 v[104:107], a[208:211], v[140:143], v[104:107]\n"
    "v_rcp_f32 v11, v11\n"
    "v_mfma_f32_16x16x32_bf16 v[108:111], a[212:215], v[140:143], v[108:111]\n"
    "s_waitcnt lgkmcnt(3)\n"
    "buffer_load_dwordx4 a[100:103], v165, s[52:55], s44 offen offset:1024 sc1\n"
    "v_mul_f32 v74, v74, v86\n"
    "v_mfma_f32_16x16x32_bf16 v[16:19], v[120:123], v[120:123], v[16:19]\n"
    "v_mul_f32 v75, v75, v87\n"
    "v_mul_f32 v72, v72, v8\n"
    "v_mfma_f32_16x16x32_bf16 v[20:23], v[144:147], v[120:123], v[20:23]\n"
    "v_mul_f32 v73, v73, v9\n"
    "v_mul_f32 v74, v74, v10\n"
    "v_mfma_f32_16x16x32_bf16 v[88:91], a[216:219], v[120:123], v[88:91]\n"
    "buffer_load_dwordx4 a[104:107], v165, s[52:55], s44 offen offset:2048 sc1\n"
    "v_mul_f32 v75, v75, v11\n"
    "v_mfma_f32_16x16x32_bf16 v[92:95], a[220:223], v[120:123], v[92:95]\n"
    "v_cvt_pk_bf16_f32 v6, v72, v73\n"
    "v_cvt_pk_bf16_f32 v7, v74, v75\n"
    "v_mfma_f32_16x16x32_bf16 v[96:99], a[224:227], v[120:123], v[96:99]\n"
    "s_waitcnt lgkmcnt(2)\n"
    "buffer_store_dwordx2 v[6:7], v177, s[52:55], s9 offen offset:2048 sc1\n"
    "v_mfma_f32_16x16x32_bf16 v[100:103], a[216:219], v[124:127], v[100:103]\n"
    "v_cndmask_b32 v21, v16, v17, s[32:33]\n"
    "v_cndmask_b32 v21, v21, v18, s[34:35]\n"
    "v_mfma_f32_16x16x32_bf16 v[104:107], a[220:223], v[124:127], v[104:107]\n"
    "v_cndmask_b32 v21, v21, v19, s[36:37]\n"
    "ds_write_b64 v174, v[20:21] offset:2048\n"
    "v_mfma_f32_16x16x32_bf16 v[108:111], a[224:227], v[124:127], v[108:111]\n"
    "s_waitcnt lgkmcnt(2)\n"
    "s_waitcnt lgkmcnt(0)\n"
    "s_barrier\n"
    "v_mfma_f32_16x16x32_bf16 v[88:91], a[228:231], v[128:131], v[88:91]\n"
    "v_mfma_f32_16x16x32_bf16 v[92:95], a[232:235], v[128:131], v[92:95]\n"
    "ds_read_b128 v[8:11], v175 offset:2048\n"
    "v_mfma_f32_16x16x32_bf16 v[96:99], a[236:239], v[128:131], v[96:99]\n"
    "buffer_load_dwordx4 a[108:111], v165, s[52:55], s44 offen offset:3072 sc1\n"
    "v_mfma_f32_16x16x32_bf16 v[100:103], a[228:231], v[132:135], v[100:103]\n"
    "s_add_u32 s44, s30, 98304\n"
    "v_mfma_f32_16x16x32_bf16 v[104:107], a[232:235], v[132:135], v[104:107]\n"
    "v_mfma_f32_16x16x32_bf16 v[108:111], a[236:239], v[132:135], v[108:111]\n"
    "buffer_load_dwordx4 a[172:175], v165, s[52:55], s44 offen offset:0 sc1\n"
    "s_waitcnt lgkmcnt(0)\n"
    "v_add_f32 v8, v8, v10\n"
    "buffer_load_dwordx4 a[176:179], v165, s[52:55], s44 offen offset:1024 sc1\n"
    "v_add_f32 v9, v9, v11\n"
    "buffer_load_dwordx4 a[180:183], v165, s[52:55], s44 offen offset:2048 sc1\n"
    "v_mul_f32 v8, 0x3acccccd, v8\n"
    "buffer_load_dwordx4 a[184:187], v165, s[52:55], s44 offen offset:3072 sc1\n"
    "v_mul_f32 v9, 0x3acccccd, v9\n"
    "s_add_u32 s44, s30, 102400\n"
    "buffer_load_dwordx4 a[188:191], v165, s[52:55], s44 offen offset:0 sc1\n"
    "v_fma_f32 v9, -v8, v8, v9\n"
    "v_max_f32 v9, 0, v9\n"
    "v_cvt_pk_bf16_f32 v8, -v8, v9\n"
    "buffer_load_dwordx4 a[192:195], v165, s[52:55], s44 offen offset:1024 sc1\n"
    "v_and_b32 v9, 0xffff0000, v8\n"
    "v_lshlrev_b32 v112, 16, v8\n"
    "buffer_load_dwordx4 a[196:199], v165, s[52:55], s44 offen offset:2048 sc1\n"
    "v_add_f32 v9, 0x3727c5ac, v9\n"
    "v_cvt_pk_bf16_f32 v9, v9, v9\n"
    "buffer_load_dwordx4 a[200:203], v165, s[52:55], s44 offen offset:3072 sc1\n"
    "v_and_b32 v9, 0xffff0000, v9\n"
    "s_add_u32 s44, s30, 106496\n"
    "buffer_load_dwordx4 a[204:207], v165, s[52:55], s44 offen offset:0 sc1\n"
    "v_sqrt_f32 v9, v9\n"
    "s_nop 0\n"
    "v_cvt_pk_bf16_f32 v9, v9, v9\n"
    "buffer_load_dwordx4 a[208:211], v165, s[52:55], s44 offen offset:1024 sc1\n"
    "v_and_b32 v9, 0xffff0000, v9\n"
    "buffer_load_dwordx4 a[212:215], v165, s[52:55], s44 offen offset:2048 sc1\n"
    "v_rcp_f32 v10, v9\n"
    "buffer_load_dwordx4 a[216:219], v165, s[52:55], s44 offen offset:3072 sc1\n"
    "v_mul_f32 v115, v9, v9\n"
    "v_mul_f32 v11, v10, v10\n"
    "v_mul_f32 v113, 0xc0135761, v10\n"
    "v_mul_f32 v11, 0x3d372713, v11\n"
    "v_mul_f32 v114, v11, v113\n"
    "v_permlane32_swap_b32 v88, v100\n"
    "v_permlane32_swap_b32 v89, v101\n"
    "v_permlane32_swap_b32 v90, v102\n"
    "v_permlane32_swap_b32 v91, v103\n"
    "v_permlane32_swap_b32 v92, v104\n"
    "v_permlane32_swap_b32 v93, v105\n"
    "v_permlane32_swap_b32 v94, v106\n"
    "v_permlane32_swap_b32 v95, v107\n"
    "v_permlane32_swap_b32 v96, v108\n"
    "v_permlane32_swap_b32 v97, v109\n"
    "v_permlane32_swap_b32 v98, v110\n"
    "v_permlane32_swap_b32 v99, v111\n"
    "v_pk_fma_f32 v[88:89], v[112:113], v[148:149], v[88:89] op_sel:[0,0,0] op_sel_hi:[0,1,1]\n"
    "v_pk_fma_f32 v[90:91], v[112:113], v[150:151], v[90:91] op_sel:[0,0,0] op_sel_hi:[0,1,1]\n"
    "v_pk_fma_f32 v[104:105], v[112:113], v[184:185], v[104:105] op_sel:[0,0,0] op_sel_hi:[0,1,1]\n"
    "v_pk_fma_f32 v[106:107], v[112:113], v[186:187], v[106:107] op_sel:[0,0,0] op_sel_hi:[0,1,1]\n"
    "v_pk_mul_f32 v[8:9], v[88:89], v[88:89]\n"
    "v_pk_mul_f32 v[10:11], v[90:91], v[90:91]\n"
    "v_pk_fma_f32 v[8:9], v[8:9], v[114:115], v[112:113] op_sel:[0,0,1] op_sel_hi:[1,0,1]\n"
    "v_pk_fma_f32 v[10:11], v[10:11], v[114:115], v[112:113] op_sel:[0,0,1] op_sel_hi:[1,0,1]\n"
    "v_pk_mul_f32 v[8:9], v[8:9], v[88:89]\n"
    "v_pk_mul_f32 v[10:11], v[10:11], v[90:91]\n"
    "v_exp_f32 v8, v8\n"
    "v_exp_f32 v9, v9\n"
    "v_exp_f32 v10, v10\n"
    "v_exp_f32 v11, v11\n"
    "v_pk_mul_f32 v[88:89], v[88:89], v[104:105]\n"
    "v_pk_mul_f32 v[90:91], v[90:91], v[106:107]\n"
    "v_pk_fma_f32 v[8:9], v[8:9], v[114:115], v[114:115] op_sel:[0,1,1] op_sel_hi:[1,1,1]\n"
    "v_pk_fma_f32 v[10:11], v[10:11], v[114:115], v[114:115] op_sel:[0,1,1] op_sel_hi:[1,1,1]\n"
    "v_rcp_f32 v8, v8\n"
    "v_rcp_f32 v9, v9\n"
    "v_rcp_f32 v10, v10\n"
    "v_rcp_f32 v11, v11\n"
    "v_pk_mul_f32 v[88:89], v[88:89], v[8:9]\n"
    "v_pk_mul_f32 v[90:91], v[90:91], v[10:11]\n"
    "v_cvt_pk_bf16_f32 v2, v88, v89\n"
    "v_cvt_pk_bf16_f32 v3, v90, v91\n"
    "s_add_u32 s9, s38, 294912\n"
    "v_pk_fma_f32 v[92:93], v[112:113], v[152:153], v[92:93] op_sel:[0,0,0] op_sel_hi:[0,1,1]\n"
    "v_pk_fma_f32 v[94:95], v[112:113], v[154:155], v[94:95] op_sel:[0,0,0] op_sel_hi:[0,1,1]\n"
    "v_pk_fma_f32 v[100:101], v[112:113], v[188:189], v[100:101] op_sel:[0,0,0] op_sel_hi:[0,1,1]\n"
    "v_pk_fma_f32 v[102:103], v[112:113], v[190:191], v[102:103] op_sel:[0,0,0] op_sel_hi:[0,1,1]\n"
    "v_pk_mul_f32 v[8:9], v[92:93], v[92:93]\n"
    "v_pk_mul_f32 v[10:11], v[94:95], v[94:95]\n"
    "v_pk_fma_f32 v[8:9], v[8:9], v[114:115], v[112:113] op_sel:[0,0,1] op_sel_hi:[1,0,1]\n"
    "v_pk_fma_f32 v[10:11], v[10:11], v[114:115], v[112:113] op_sel:[0,0,1] op_sel_hi:[1,0,1]\n"
    "v_pk_mul_f32 v[8:9], v[8:9], v[92:93]\n"
    "v_pk_mul_f32 v[10:11], v[10:11], v[94:95]\n"
    "v_exp_f32 v8, v8\n"
    "v_exp_f32 v9, v9\n"
    "v_exp_f32 v10, v10\n"
    "v_exp_f32 v11, v11\n"
    "v_pk_mul_f32 v[92:93], v[92:93], v[100:101]\n"
    "v_pk_mul_f32 v[94:95], v[94:95], v[102:103]\n"
    "v_pk_fma_f32 v[8:9], v[8:9], v[114:115], v[114:115] op_sel:[0,1,1] op_sel_hi:[1,1,1]\n"
    "v_pk_fma_f32 v[10:11], v[10:11], v[114:115], v[114:115] op_sel:[0,1,1] op_sel_hi:[1,1,1]\n"
    "v_rcp_f32 v8, v8\n"
    "v_rcp_f32 v9, v9\n"
    "v_rcp_f32 v10, v10\n"
    "v_rcp_f32 v11, v11\n"
    "v_pk_mul_f32 v[92:93], v[92:93], v[8:9]\n"
    "v_pk_mul_f32 v[94:95], v[94:95], v[10:11]\n"
    "v_cvt_pk_bf16_f32 v4, v92, v93\n"
    "v_cvt_pk_bf16_f32 v5, v94, v95\n"
    "buffer_store_dwordx4 v[2:5], v176, s[52:55], s9 offen sc1\n"
    "v_pk_fma_f32 v[96:97], v[112:113], v[156:157], v[96:97] op_sel:[0,0,0] op_sel_hi:[0,1,1]\n"
    "v_pk_fma_f32 v[98:99], v[112:113], v[158:159], v[98:99] op_sel:[0,0,0] op_sel_hi:[0,1,1]\n"
    "v_pk_fma_f32 v[108:109], v[112:113], v[192:193], v[108:109] op_sel:[0,0,0] op_sel_hi:[0,1,1]\n"
    "v_pk_fma_f32 v[110:111], v[112:113], v[194:195], v[110:111] op_sel:[0,0,0] op_sel_hi:[0,1,1]\n"
    "v_pk_mul_f32 v[8:9], v[96:97], v[96:97]\n"
    "v_pk_mul_f32 v[10:11], v[98:99], v[98:99]\n"
    "v_pk_fma_f32 v[8:9], v[8:9], v[114:115], v[112:113] op_sel:[0,0,1] op_sel_hi:[1,0,1]\n"
    "v_pk_fma_f32 v[10:11], v[10:11], v[114:115], v[112:113] op_sel:[0,0,1] op_sel_hi:[1,0,1]\n"
    "v_pk_mul_f32 v[8:9], v[8:9], v[96:97]\n"
    "v_pk_mul_f32 v[10:11], v[10:11], v[98:99]\n"
    "v_exp_f32 v8, v8\n"
    "v_exp_f32 v9, v9\n"
    "v_exp_f32 v10, v10\n"
    "v_exp_f32 v11, v11\n"
    "v_pk_mul_f32 v[96:97], v[96:97], v[108:109]\n"
    "v_pk_mul_f32 v[98:99], v[98:99], v[110:111]\n"
    "v_pk_fma_f32 v[8:9], v[8:9], v[114:115], v[114:115] op_sel:[0,1,1] op_sel_hi:[1,1,1]\n"
    "v_pk_fma_f32 v[10:11], v[10:11], v[114:115], v[114:115] op_sel:[0,1,1] op_sel_hi:[1,1,1]\n"
    "v_rcp_f32 v8, v8\n"
    "v_rcp_f32 v9, v9\n"
    "v_rcp_f32 v10, v10\n"
    "v_rcp_f32 v11, v11\n"
    "v_pk_mul_f32 v[96:97], v[96:97], v[8:9]\n"
    "v_pk_mul_f32 v[98:99], v[98:99], v[10:11]\n"
    "v_cvt_pk_bf16_f32 v6, v96, v97\n"
    "v_cvt_pk_bf16_f32 v7, v98, v99\n"
    "buffer_store_dwordx2 v[6:7], v177, s[52:55], s9 offen offset:2048 sc1\n"
    "s_waitcnt vmcnt(27)\n"
    "v_mfma_f32_16x16x32_bf16 v[144:147], v[196:199], a[64:67], 0\n"
    "v_mfma_f32_16x16x32_bf16 v[148:151], v[200:203], a[64:67], 0\n"
    "v_mfma_f32_16x16x32_bf16 v[152:155], v[204:207], a[64:67], 0\n"
    "v_mfma_f32_16x16x32_bf16 v[156:159], v[208:211], a[64:67], 0\n"
    "v_mfma_f32_16x16x32_bf16 v[184:187], v[212:215], a[64:67], 0\n"
    "s_waitcnt vmcnt(26)\n"
    "v_mfma_f32_16x16x32_bf16 v[144:147], v[216:219], a[68:71], v[144:147]\n"
    "v_mfma_f32_16x16x32_bf16 v[148:151], v[220:223], a[68:71], v[148:151]\n"
    "v_mfma_f32_16x16x32_bf16 v[152:155], v[224:227], a[68:71], v[152:155]\n"
    "v_mfma_f32_16x16x32_bf16 v[156:159], v[228:231], a[68:71], v[156:159]\n"
    "v_mfma_f32_16x16x32_bf16 v[184:187], v[232:235], a[68:71], v[184:187]\n"
    "s_waitcnt vmcnt(25)\n"
    "v_mfma_f32_16x16x32_bf16 v[144:147], v[236:239], a[72:75], v[144:147]\n"
    "v_mfma_f32_16x16x32_bf16 v[148:151], v[240:243], a[72:75], v[148:151]\n"
    "v_mfma_f32_16x16x32_bf16 v[152:155], v[244:247], a[72:75], v[152:155]\n"
    "v_mfma_f32_16x16x32_bf16 v[156:159], v[248:251], a[72:75], v[156:159]\n"
    "v_mfma_f32_16x16x32_bf16 v[184:187], v[252:255], a[72:75], v[184:187]\n"
    "s_waitcnt vmcnt(23)\n"
    "v_mfma_f32_16x16x32_bf16 v[144:147], a[240:243], a[76:79], v[144:147]\n"
    "v_mfma_f32_16x16x32_bf16 v[148:151], a[244:247], a[76:79], v[148:151]\n"
    "v_mfma_f32_16x16x32_bf16 v[152:155], a[248:251], a[76:79], v[152:155]\n"
    "v_mfma_f32_16x16x32_bf16 v[156:159], a[252:255], a[76:79], v[156:159]\n"
    "v_mfma_f32_16x16x32_bf16 v[184:187], v[24:27], a[76:79], v[184:187]\n"
    "s_waitcnt vmcnt(22)\n"
    "v_mfma_f32_16x16x32_bf16 v[144:147], v[28:31], a[80:83], v[144:147]\n"
    "v_mfma_f32_16x16x32_bf16 v[148:151], v[32:35], a[80:83], v[148:151]\n"
    "v_mfma_f32_16x16x32_bf16 v[152:155], v[36:39], a[80:83], v[152:155]\n"
    "v_mfma_f32_16x16x32_bf16 v[156:159], v[40:43], a[80:83], v[156:159]\n"
    "v_mfma_f32_16x16x32_bf16 v[184:187], v[44:47], a[80:83], v[184:187]\n"
    "s_waitcnt vmcnt(21)\n"
    "v_mfma_f32_16x16x32_bf16 v[144:147], v[48:51], a[84:87], v[144:147]\n"
    "v_mfma_f32_16x16x32_bf16 v[148:151], v[52:55], a[84:87], v[148:151]\n"
    "v_mfma_f32_16x16x32_bf16 v[152:155], v[56:59], a[84:87], v[152:155]\n"
    "v_mfma_f32_16x16x32_bf16 v[156:159], v[60:63], a[84:87], v[156:159]\n"
    "v_mfma_f32_16x16x32_bf16 v[184:187], a[0:3], a[84:87], v[184:187]\n"
    "s_waitcnt vmcnt(20)\n"
    "v_mfma_f32_16x16x32_bf16 v[144:147], a[4:7], a[88:91], v[144:147]\n"
    "v_mfma_f32_16x16x32_bf16 v[148:151], a[8:11], a[88:91], v[148:151]\n"
    "v_mfma_f32_16x16x32_bf16 v[152:155], a[12:15], a[88:91], v[152:155]\n"
    "v_mfma_f32_16x16x32_bf16 v[156:159], a[16:19], a[88:91], v[156:159]\n"
    "v_mfma_f32_16x16x32_bf16 v[184:187], a[20:23], a[88:91], v[184:187]\n"
    "s_waitcnt vmcnt(19)\n"
    "v_mfma_f32_16x16x32_bf16 v[144:147], a[24:27], a[92:95], v[144:147]\n"
    "v_mfma_f32_16x16x32_bf16 v[148:151], a[28:31], a[92:95], v[148:151]\n"
    "v_mfma_f32_16x16x32_bf16 v[152:155], a[32:35], a[92:95], v[152:155]\n"
    "v_mfma_f32_16x16x32_bf16 v[156:159], a[36:39], a[92:95], v[156:159]\n"
    "v_mfma_f32_16x16x32_bf16 v[184:187], a[40:43], a[92:95], v[184:187]\n"
    "s_waitcnt vmcnt(18)\n"
    "v_mfma_f32_16x16x32_bf16 v[144:147], a[44:47], a[96:99], v[144:147]\n"
    "v_mfma_f32_16x16x32_bf16 v[148:151], a[48:51], a[96:99], v[148:151]\n"
    "v_mfma_f32_16x16x32_bf16 v[152:155], a[52:55], a[96:99], v[152:155]\n"
    "v_mfma_f32_16x16x32_bf16 v[156:159], a[56:59], a[96:99], v[156:159]\n"
    "v_mfma_f32_16x16x32_bf16 v[184:187], a[60:63], a[96:99], v[184:187]\n"
    "s_waitcnt vmcnt(17)\n"
    "v_mfma_f32_16x16x32_bf16 v[144:147], a[112:115], a[100:103], v[144:147]\n"
    "s_waitcnt vmcnt(0)\n"
    "s_barrier\n"
    "v_mov_b32 v116, s64\n"
    "v_mov_b32 v117, s65\n"
    "v_cvt_f32_u32 v118, v0\n"
    "s_add_u32 s98, s5, 262144\n"
    "buffer_store_dwordx2 v[116:117], v118, s[52:55], s98 offen offset:0 sc1\n"
    "v_mfma_f32_16x16x32_bf16 v[148:151], a[116:119], a[100:103], v[148:151]\n"
    "buffer_load_dwordx2 v[160:161], v163, s[52:55], s29 offen sc1\n"
    "v_mfma_f32_16x16x32_bf16 v[152:155], a[120:123], a[100:103], v[152:155]\n"
    "v_mfma_f32_16x16x32_bf16 v[156:159], a[124:127], a[100:103], v[156:159]\n"
    "v_mfma_f32_16x16x32_bf16 v[184:187], a[128:131], a[100:103], v[184:187]\n"
    "s_add_u32 s10, s31, 0\n"
    "buffer_load_dwordx2 v[2:3], v167, s[52:55], s10 offen offset:0 sc1\n"
    "buffer_load_dwordx2 v[4:5], v167, s[52:55], s10 offen offset:256 sc1\n"
    "v_mfma_f32_16x16x32_bf16 v[144:147], a[132:135], a[104:107], v[144:147]\n"
    "v_mfma_f32_16x16x32_bf16 v[148:151], a[136:139], a[104:107], v[148:151]\n"
    "v_mfma_f32_16x16x32_bf16 v[152:155], a[140:143], a[104:107], v[152:155]\n"
    "s_add_u32 s10, s31, 40960\n"
    "buffer_load_dwordx2 v[6:7], v167, s[52:55], s10 offen offset:0 sc1\n"
    "buffer_load_dwordx2 v[8:9], v167, s[52:55], s10 offen offset:256 sc1\n"
    "v_mfma_f32_16x16x32_bf16 v[156:159], a[144:147], a[104:107], v[156:159]\n"
    "v_mfma_f32_16x16x32_bf16 v[184:187], a[148:151], a[104:107], v[184:187]\n"
    "v_mfma_f32_16x16x32_bf16 v[144:147], a[152:155], a[108:111], v[144:147]\n"
    "s_add_u32 s10, s31, 81920\n"
    "buffer_load_dwordx2 v[172:173], v167, s[52:55], s10 offen offset:0 sc1\n"
    "buffer_load_dwordx2 v[174:175], v167, s[52:55], s10 offen offset:256 sc1\n"
    "v_mfma_f32_16x16x32_bf16 v[148:151], a[156:159], a[108:111], v[148:151]\n"
    "v_mfma_f32_16x16x32_bf16 v[152:155], a[160:163], a[108:111], v[152:155]\n"
    "v_mfma_f32_16x16x32_bf16 v[156:159], a[164:167], a[108:111], v[156:159]\n"
    "v_mfma_f32_16x16x32_bf16 v[184:187], a[168:171], a[108:111], v[184:187]\n"
    "v_mfma_f32_16x16x32_bf16 v[188:191], v[196:199], a[172:175], 0\n"
    "v_mfma_f32_16x16x32_bf16 v[192:195], v[200:203], a[172:175], 0\n"
    "s_add_u32 s10, s31, 122880\n"
    "buffer_load_dwordx2 v[180:181], v167, s[52:55], s10 offen offset:0 sc1\n"
    "buffer_load_dwordx2 v[182:183], v167, s[52:55], s10 offen offset:256 sc1\n"
    "v_mfma_f32_16x16x32_bf16 v[12:15], v[204:207], a[172:175], 0\n"
    "v_mfma_f32_16x16x32_bf16 v[16:19], v[208:211], a[172:175], 0\n"
    "v_mfma_f32_16x16x32_bf16 v[20:23], v[212:215], a[172:175], 0\n"
    "v_mfma_f32_16x16x32_bf16 v[188:191], v[216:219], a[176:179], v[188:191]\n"
    "v_mfma_f32_16x16x32_bf16 v[192:195], v[220:223], a[176:179], v[192:195]\n"
    "ds_write_b128 v169, v[144:147] offset:0\n"
    "v_mfma_f32_16x16x32_bf16 v[12:15], v[224:227], a[176:179], v[12:15]\n"
    "v_mfma_f32_16x16x32_bf16 v[16:19], v[228:231], a[176:179], v[16:19]\n"
    "v_mfma_f32_16x16x32_bf16 v[20:23], v[232:235], a[176:179], v[20:23]\n"
    "ds_write_b128 v169, v[148:151] offset:1024\n"
    "v_mfma_f32_16x16x32_bf16 v[188:191], v[236:239], a[180:183], v[188:191]\n"
    "v_mfma_f32_16x16x32_bf16 v[192:195], v[240:243], a[180:183], v[192:195]\n"
    "v_mfma_f32_16x16x32_bf16 v[12:15], v[244:247], a[180:183], v[12:15]\n"
    "ds_write_b128 v169, v[152:155] offset:2048\n"
    "v_mfma_f32_16x16x32_bf16 v[16:19], v[248:251], a[180:183], v[16:19]\n"
    "v_mfma_f32_16x16x32_bf16 v[20:23], v[252:255], a[180:183], v[20:23]\n"
    "v_mfma_f32_16x16x32_bf16 v[188:191], a[240:243], a[184:187], v[188:191]\n"
    "ds_write_b128 v169, v[156:159] offset:3072\n"
    "v_mfma_f32_16x16x32_bf16 v[192:195], a[244:247], a[184:187], v[192:195]\n"
    "v_mfma_f32_16x16x32_bf16 v[12:15], a[248:251], a[184:187], v[12:15]\n"
    "v_mfma_f32_16x16x32_bf16 v[16:19], a[252:255], a[184:187], v[16:19]\n"
    "ds_write_b128 v169, v[184:187] offset:4096\n"
    "v_mfma_f32_16x16x32_bf16 v[20:23], v[24:27], a[184:187], v[20:23]\n"
    "v_mfma_f32_16x16x32_bf16 v[188:191], v[28:31], a[188:191], v[188:191]\n"
    "s_waitcnt lgkmcnt(0)\n"
    "s_barrier\n"
    "v_mfma_f32_16x16x32_bf16 v[192:195], v[32:35], a[188:191], v[192:195]\n"
    "v_mfma_f32_16x16x32_bf16 v[12:15], v[36:39], a[188:191], v[12:15]\n"
    "v_mfma_f32_16x16x32_bf16 v[16:19], v[40:43], a[188:191], v[16:19]\n"
    "v_mfma_f32_16x16x32_bf16 v[20:23], v[44:47], a[188:191], v[20:23]\n"
    "v_mfma_f32_16x16x32_bf16 v[188:191], v[48:51], a[192:195], v[188:191]\n"
    "v_mfma_f32_16x16x32_bf16 v[192:195], v[52:55], a[192:195], v[192:195]\n"
    "v_mfma_f32_16x16x32_bf16 v[12:15], v[56:59], a[192:195], v[12:15]\n"
    "v_mfma_f32_16x16x32_bf16 v[16:19], v[60:63], a[192:195], v[16:19]\n"
    "v_mfma_f32_16x16x32_bf16 v[20:23], a[0:3], a[192:195], v[20:23]\n"
    "ds_read_b128 v[88:91], v166 offset:0\n"
    "v_mfma_f32_16x16x32_bf16 v[188:191], a[4:7], a[196:199], v[188:191]\n"
    "ds_read_b128 v[92:95], v166 offset:256\n"
    "v_mfma_f32_16x16x32_bf16 v[192:195], a[8:11], a[196:199], v[192:195]\n"
    "ds_read_b128 v[96:99], v166 offset:5120\n"
    "v_mfma_f32_16x16x32_bf16 v[12:15], a[12:15], a[196:199], v[12:15]\n"
    "ds_read_b128 v[100:103], v166 offset:5376\n"
    "v_mfma_f32_16x16x32_bf16 v[16:19], a[16:19], a[196:199], v[16:19]\n"
    "ds_read_b128 v[104:107], v166 offset:10240\n"
    "v_mfma_f32_16x16x32_bf16 v[20:23], a[20:23], a[196:199], v[20:23]\n"
    "ds_read_b128 v[108:111], v166 offset:10496\n"
    "v_mfma_f32_16x16x32_bf16 v[188:191], a[24:27], a[200:203], v[188:191]\n"
    "s_waitcnt vmcnt(8)\n"
    "v_cmp_ne_u64 vcc, s[64:65], v[160:161]\n"
    "s_cmp_eq_u64 vcc, 0\n"
    "s_cbranch_scc1 .L39_%=\n"
    "s_mov_b32 s98, 100000\n"
    ".L41_%=:\n"
    "buffer_load_dwordx2 v[160:161], v163, s[52:55], s29 offen sc1\n"
    "s_waitcnt vmcnt(0)\n"
    "v_cmp_ne_u64 vcc, s[64:65], v[160:161]\n"
    "s_cmp_eq_u64 vcc, 0\n"
    "s_cbranch_scc1 .L39_%=\n"
    "s_sub_u32 s98, s98, 1\n"
    "s_cbranch_scc0 .L41_%=\n"
    ".L39_%=:\n"
    "v_mfma_f32_16x16x32_bf16 v[192:195], a[28:31], a[200:203], v[192:195]\n"
    "ds_read_b128 v[112:115], v166 offset:15360\n"
    "s_add_u32 s44, s30, 196608\n"
    "v_mfma_f32_16x16x32_bf16 v[12:15], a[32:35], a[200:203], v[12:15]\n"
    "buffer_load_dwordx4 v[64:67], v165, s[52:55], s44 offen offset:0 sc1\n"
    "v_mfma_f32_16x16x32_bf16 v[16:19], a[36:39], a[200:203], v[16:19]\n"
    "ds_read_b128 v[116:119], v166 offset:15616\n"
    "v_mfma_f32_16x16x32_bf16 v[20:23], a[40:43], a[200:203], v[20:23]\n"
    "v_mfma_f32_16x16x32_bf16 v[188:191], a[44:47], a[204:207], v[188:191]\n"
    "buffer_load_dwordx4 v[68:71], v165, s[52:55], s44 offen offset:1024 sc1\n"
    "v_mfma_f32_16x16x32_bf16 v[192:195], a[48:51], a[204:207], v[192:195]\n"
    "s_waitcnt lgkmcnt(5)\n"
    "v_add_f32 v88, v88, v96\n"
    "v_add_f32 v89, v89, v97\n"
    "v_add_f32 v90, v90, v98\n"
    "v_add_f32 v91, v91, v99\n"
    "v_mfma_f32_16x16x32_bf16 v[12:15], a[52:55], a[204:207], v[12:15]\n"
    "s_waitcnt lgkmcnt(4)\n"
    "v_add_f32 v92, v92, v100\n"
    "v_add_f32 v93, v93, v101\n"
    "v_add_f32 v94, v94, v102\n"
    "v_add_f32 v95, v95, v103\n"
    "v_mfma_f32_16x16x32_bf16 v[16:19], a[56:59], a[204:207], v[16:19]\n"
    "buffer_load_dwordx4 v[72:75], v165, s[52:55], s44 offen offset:2048 sc1\n"
    "v_mfma_f32_16x16x32_bf16 v[20:23], a[60:63], a[204:207], v[20:23]\n"
    "s_waitcnt lgkmcnt(1)\n"
    "v_add_f32 v104, v104, v112\n"
    "v_add_f32 v105, v105, v113\n"
    "v_add_f32 v106, v106, v114\n"
    "v_add_f32 v107, v107, v115\n"
    "v_mfma_f32_16x16x32_bf16 v[188:191], a[112:115], a[208:211], v[188:191]\n"
    "s_waitcnt lgkmcnt(0)\n"
    "v_add_f32 v108, v108, v116\n"
    "v_add_f32 v109, v109, v117\n"
    "v_add_f32 v110, v110, v118\n"
    "v_add_f32 v111, v111, v119\n"
    "v_mfma_f32_16x16x32_bf16 v[192:195], a[116:119], a[208:211], v[192:195]\n"
    "buffer_load_dwordx4 v[76:79], v165, s[52:55], s44 offen offset:3072 sc1\n"
    "s_add_u32 s44, s30, 200704\n"
    "buffer_load_dwordx4 v[80:83], v165, s[52:55], s44 offen offset:0 sc1\n"
    "buffer_load_dwordx4 v[84:87], v165, s[52:55], s44 offen offset:1024 sc1\n"
    "buffer_load_dwordx4 v[120:123], v165, s[52:55], s44 offen offset:2048 sc1\n"
    "buffer_load_dwordx4 v[124:127], v165, s[52:55], s44 offen offset:3072 sc1\n"
    "s_add_u32 s44, s30, 204800\n"
    "buffer_load_dwordx4 v[128:131], v165, s[52:55], s44 offen offset:0 sc1\n"
    "buffer_load_dwordx4 v[132:135], v165, s[52:55], s44 offen offset:1024 sc1\n"
    "buffer_load_dwordx4 v[136:139], v165, s[52:55], s44 offen offset:2048 sc1\n"
    "buffer_load_dwordx4 v[140:143], v165, s[52:55], s44 offen offset:3072 sc1\n"
    "v_mfma_f32_16x16x32_bf16 v[12:15], a[120:123], a[208:211], v[12:15]\n"
    "s_add_u32 s44, s30, 294912\n"
    "v_mfma_f32_16x16x32_bf16 v[16:19], a[124:127], a[208:211], v[16:19]\n"
    "s_waitcnt vmcnt(19)\n"
    "v_lshlrev_b32 v96, 16, v2\n"
    "v_and_b32 v97, 0xffff0000, v2\n"
    "v_lshlrev_b32 v98, 16, v3\n"
    "v_mfma_f32_16x16x32_bf16 v[20:23], a[128:131], a[208:211], v[20:23]\n"
    "v_and_b32 v99, 0xffff0000, v3\n"
    "v_mfma_f32_16x16x32_bf16 v[188:191], a[132:135], a[212:215], v[188:191]\n"
    "s_waitcnt vmcnt(18)\n"
    "v_lshlrev_b32 v100, 16, v4\n"
    "v_and_b32 v101, 0xffff0000, v4\n"
    "v_lshlrev_b32 v102, 16, v5\n"
    "v_and_b32 v103, 0xffff0000, v5\n"
    "v_mfma_f32_16x16x32_bf16 v[192:195], a[136:139], a[212:215], v[192:195]\n"
    "v_add_f32 v88, v88, v104\n"
    "v_add_f32 v89, v89, v105\n"
    "v_add_f32 v90, v90, v106\n"
    "v_add_f32 v91, v91, v107\n"
    "v_mfma_f32_16x16x32_bf16 v[12:15], a[140:143], a[212:215], v[12:15]\n"
    "v_add_f32 v92, v92, v108\n"
    "v_add_f32 v93, v93, v109\n"
    "v_add_f32 v94, v94, v110\n"
    "v_add_f32 v95, v95, v111\n"
    "v_mfma_f32_16x16x32_bf16 v[16:19], a[144:147], a[212:215], v[16:19]\n"
    "v_add_f32 v88, v88, v96\n"
    "v_add_f32 v89, v89, v97\n"
    "v_add_f32 v90, v90, v98\n"
    "v_add_f32 v91, v91, v99\n"
    "v_add_f32 v92, v92, v100\n"
    "v_mfma_f32_16x16x32_bf16 v[20:23], a[148:151], a[212:215], v[20:23]\n"
    "v_add_f32 v93, v93, v101\n"
    "v_add_f32 v94, v94, v102\n"
    "v_add_f32 v95, v95, v103\n"
    "v_cvt_pk_bf16_f32 v96, v88, v89\n"
    "v_cvt_pk_bf16_f32 v97, v90, v91\n"
    "v_cvt_pk_bf16_f32 v98, v92, v93\n"
    "v_mfma_f32_16x16x32_bf16 v[188:191], a[152:155], a[216:219], v[188:191]\n"
    "v_cvt_pk_bf16_f32 v99, v94, v95\n"
    "s_add_u32 s9, s42, 0\n"
    "v_mfma_f32_16x16x32_bf16 v[192:195], a[156:159], a[216:219], v[192:195]\n"
    "buffer_store_dwordx4 v[96:99], v168, s[56:59], s9 offen\n"
    "v_mfma_f32_16x16x32_bf16 v[12:15], a[160:163], a[216:219], v[12:15]\n"
    "v_mfma_f32_16x16x32_bf16 v[16:19], a[164:167], a[216:219], v[16:19]\n"
    "buffer_load_dwordx4 a[64:67], v165, s[52:55], s44 offen offset:0 sc1\n"
    "buffer_load_dwordx4 a[68:71], v165, s[52:55], s44 offen offset:1024 sc1\n"
    "buffer_load_dwordx4 a[72:75], v165, s[52:55], s44 offen offset:2048 sc1\n"
    "buffer_load_dwordx4 a[76:79], v165, s[52:55], s44 offen offset:3072 sc1\n"
    "s_add_u32 s44, s30, 299008\n"
    "buffer_load_dwordx4 a[80:83], v165, s[52:55], s44 offen offset:0 sc1\n"
    "buffer_load_dwordx4 a[84:87], v165, s[52:55], s44 offen offset:1024 sc1\n"
    "buffer_load_dwordx4 a[88:91], v165, s[52:55], s44 offen offset:2048 sc1\n"
    "buffer_load_dwordx4 a[92:95], v165, s[52:55], s44 offen offset:3072 sc1\n"
    "s_add_u32 s44, s30, 303104\n"
    "buffer_load_dwordx4 a[96:99], v165, s[52:55], s44 offen offset:0 sc1\n"
    "buffer_load_dwordx4 a[100:103], v165, s[52:55], s44 offen offset:1024 sc1\n"
    "buffer_load_dwordx4 a[104:107], v165, s[52:55], s44 offen offset:2048 sc1\n"
    "buffer_load_dwordx4 a[108:111], v165, s[52:55], s44 offen offset:3072 sc1\n"
    "v_mfma_f32_16x16x32_bf16 v[20:23], a[168:171], a[216:219], v[20:23]\n"
    "s_waitcnt vmcnt(24)\n"
    "v_mfma_f32_16x16x32_bf16 v[144:147], v[196:199], v[64:67], 0\n"
    "v_mfma_f32_16x16x32_bf16 v[148:151], v[200:203], v[64:67], 0\n"
    "v_mfma_f32_16x16x32_bf16 v[152:155], v[204:207], v[64:67], 0\n"
    "v_mfma_f32_16x16x32_bf16 v[156:159], v[208:211], v[64:67], 0\n"
    "v_mfma_f32_16x16x32_bf16 v[184:187], v[212:215], v[64:67], 0\n"
    "s_waitcnt vmcnt(23)\n"
    "v_mfma_f32_16x16x32_bf16 v[144:147], v[216:219], v[68:71], v[144:147]\n"
    "v_mfma_f32_16x16x32_bf16 v[148:151], v[220:223], v[68:71], v[148:151]\n"
    "ds_write_b128 v169, v[188:191] offset:20480\n"
    "v_mfma_f32_16x16x32_bf16 v[152:155], v[224:227], v[68:71], v[152:155]\n"
    "v_mfma_f32_16x16x32_bf16 v[156:159], v[228:231], v[68:71], v[156:159]\n"
    "v_mfma_f32_16x16x32_bf16 v[184:187], v[232:235], v[68:71], v[184:187]\n"
    "s_waitcnt vmcnt(22)\n"
    "ds_write_b128 v169, v[192:195] offset:21504\n"
    "v_mfma_f32_16x16x32_bf16 v[144:147], v[236:239], v[72:75], v[144:147]\n"
    "v_mfma_f32_16x16x32_bf16 v[148:151], v[240:243], v[72:75], v[148:151]\n"
    "v_mfma_f32_16x16x32_bf16 v[152:155], v[244:247], v[72:75], v[152:155]\n"
    "ds_write_b128 v169, v[12:15] offset:22528\n"
    "v_mfma_f32_16x16x32_bf16 v[156:159], v[248:251], v[72:75], v[156:159]\n"
    "v_mfma_f32_16x16x32_bf16 v[184:187], v[252:255], v[72:75], v[184:187]\n"
    "s_waitcnt vmcnt(21)\n"
    "v_mfma_f32_16x16x32_bf16 v[144:147], a[240:243], v[76:79], v[144:147]\n"
    "ds_write_b128 v169, v[16:19] offset:23552\n"
    "v_mfma_f32_16x16x32_bf16 v[148:151], a[244:247], v[76:79], v[148:151]\n"
    "v_mfma_f32_16x16x32_bf16 v[152:155], a[248:251], v[76:79], v[152:155]\n"
    "v_mfma_f32_16x16x32_bf16 v[156:159], a[252:255], v[76:79], v[156:159]\n"
    "ds_write_b128 v169, v[20:23] offset:24576\n"
    "v_mfma_f32_16x16x32_bf16 v[184:187], v[24:27], v[76:79], v[184:187]\n"
    "s_waitcnt vmcnt(20)\n"
    "v_mfma_f32_16x16x32_bf16 v[144:147], v[28:31], v[80:83], v[144:147]\n"
    "s_waitcnt lgkmcnt(0)\n"
    "s_barrier\n"
    "v_mfma_f32_16x16x32_bf16 v[148:151], v[32:35], v[80:83], v[148:151]\n"
    "ds_read_b128 v[88:91], v166 offset:20480\n"
    "v_mfma_f32_16x16x32_bf16 v[152:155], v[36:39], v[80:83], v[152:155]\n"
    "ds_read_b128 v[92:95], v166 offset:20736\n"
    "v_mfma_f32_16x16x32_bf16 v[156:159], v[40:43], v[80:83], v[156:159]\n"
    "ds_read_b128 v[96:99], v166 offset:25600\n"
    "v_mfma_f32_16x16x32_bf16 v[184:187], v[44:47], v[80:83], v[184:187]\n"
    "s_waitcnt vmcnt(19)\n"
    "ds_read_b128 v[100:103], v166 offset:25856\n"
    "v_mfma_f32_16x16x32_bf16 v[144:147], v[48:51], v[84:87], v[144:147]\n"
    "ds_read_b128 v[104:107], v166 offset:30720\n"
    "v_mfma_f32_16x16x32_bf16 v[148:151], v[52:55], v[84:87], v[148:151]\n"
    "ds_read_b128 v[108:111], v166 offset:30976\n"
    "v_mfma_f32_16x16x32_bf16 v[152:155], v[56:59], v[84:87], v[152:155]\n"
    "ds_read_b128 v[112:115], v166 offset:35840\n"
    "v_mfma_f32_16x16x32_bf16 v[156:159], v[60:63], v[84:87], v[156:159]\n"
    "ds_read_b128 v[116:119], v166 offset:36096\n"
    "v_mfma_f32_16x16x32_bf16 v[184:187], a[0:3], v[84:87], v[184:187]\n"
    "s_waitcnt vmcnt(18)\n"
    "v_mfma_f32_16x16x32_bf16 v[144:147], a[4:7], v[120:123], v[144:147]\n"
    "v_mfma_f32_16x16x32_bf16 v[148:151], a[8:11], v[120:123], v[148:151]\n"
    "s_waitcnt lgkmcnt(5)\n"
    "v_add_f32 v88, v88, v96\n"
    "v_add_f32 v89, v89, v97\n"
    "v_mfma_f32_16x16x32_bf16 v[152:155], a[12:15], v[120:123], v[152:155]\n"
    "v_add_f32 v90, v90, v98\n"
    "v_add_f32 v91, v91, v99\n"
    "v_mfma_f32_16x16x32_bf16 v[156:159], a[16:19], v[120:123], v[156:159]\n"
    "s_waitcnt lgkmcnt(4)\n"
    "v_add_f32 v92, v92, v100\n"
    "v_add_f32 v93, v93, v101\n"
    "v_mfma_f32_16x16x32_bf16 v[184:187], a[20:23], v[120:123], v[184:187]\n"
    "s_waitcnt vmcnt(17)\n"
    "v_add_f32 v94, v94, v102\n"
    "v_add_f32 v95, v95, v103\n"
    "v_mfma_f32_16x16x32_bf16 v[144:147], a[24:27], v[124:127], v[144:147]\n"
    "s_waitcnt lgkmcnt(1)\n"
    "v_add_f32 v104, v104, v112\n"
    "v_add_f32 v105, v105, v113\n"
    "v_mfma_f32_16x16x32_bf16 v[148:151], a[28:31], v[124:127], v[148:151]\n"
    "v_add_f32 v106, v106, v114\n"
    "v_add_f32 v107, v107, v115\n"
    "v_mfma_f32_16x16x32_bf16 v[152:155], a[32:35], v[124:127], v[152:155]\n"
    "s_waitcnt lgkmcnt(0)\n"
    "v_add_f32 v108, v108, v116\n"
    "v_add_f32 v109, v109, v117\n"
    "v_mfma_f32_16x16x32_bf16 v[156:159], a[36:39], v[124:127], v[156:159]\n"
    "v_add_f32 v110, v110, v118\n"
    "v_add_f32 v111, v111, v119\n"
    "v_mfma_f32_16x16x32_bf16 v[184:187], a[40:43], v[124:127], v[184:187]\n"
    "s_waitcnt vmcnt(16)\n"
    "v_lshlrev_b32 v96, 16, v6\n"
    "v_and_b32 v97, 0xffff0000, v6\n"
    "v_mfma_f32_16x16x32_bf16 v[144:147], a[44:47], v[128:131], v[144:147]\n"
    "v_lshlrev_b32 v98, 16, v7\n"
    "v_and_b32 v99, 0xffff0000, v7\n"
    "v_mfma_f32_16x16x32_bf16 v[148:151], a[48:51], v[128:131], v[148:151]\n"
    "v_lshlrev_b32 v100, 16, v8\n"
    "v_and_b32 v101, 0xffff0000, v8\n"
    "v_mfma_f32_16x16x32_bf16 v[152:155], a[52:55], v[128:131], v[152:155]\n"
    "v_lshlrev_b32 v102, 16, v9\n"
    "v_and_b32 v103, 0xffff0000, v9\n"
    "v_mfma_f32_16x16x32_bf16 v[156:159], a[56:59], v[128:131], v[156:159]\n"
    "v_add_f32 v88, v88, v104\n"
    "v_add_f32 v89, v89, v105\n"
    "v_mfma_f32_16x16x32_bf16 v[184:187], a[60:63], v[128:131], v[184:187]\n"
    "s_waitcnt vmcnt(15)\n"
    "v_add_f32 v90, v90, v106\n"
    "v_add_f32 v91, v91, v107\n"
    "v_mfma_f32_16x16x32_bf16 v[144:147], a[112:115], v[132:135], v[144:147]\n"
    "v_add_f32 v92, v92, v108\n"
    "v_add_f32 v93, v93, v109\n"
    "v_mfma_f32_16x16x32_bf16 v[148:151], a[116:119], v[132:135], v[148:151]\n"
    "v_add_f32 v94, v94, v110\n"
    "v_add_f32 v95, v95, v111\n"
    "v_mfma_f32_16x16x32_bf16 v[152:155], a[120:123], v[132:135], v[152:155]\n"
    "v_add_f32 v88, v88, v96\n"
    "v_add_f32 v89, v89, v97\n"
    "v_mfma_f32_16x16x32_bf16 v[156:159], a[124:127], v[132:135], v[156:159]\n"
    "v_add_f32 v90, v90, v98\n"
    "v_add_f32 v91, v91, v99\n"
    "v_mfma_f32_16x16x32_bf16 v[184:187], a[128:131], v[132:135], v[184:187]\n"
    "s_waitcnt vmcnt(14)\n"
    "v_add_f32 v92, v92, v100\n"
    "v_add_f32 v93, v93, v101\n"
    "v_mfma_f32_16x16x32_bf16 v[144:147], a[132:135], v[136:139], v[144:147]\n"
    "v_add_f32 v94, v94, v102\n"
    "v_add_f32 v95, v95, v103\n"
    "v_mfma_f32_16x16x32_bf16 v[148:151], a[136:139], v[136:139], v[148:151]\n"
    "v_cvt_pk_bf16_f32 v96, v88, v89\n"
    "v_cvt_pk_bf16_f32 v97, v90, v91\n"
    "v_mfma_f32_16x16x32_bf16 v[152:155], a[140:143], v[136:139], v[152:155]\n"
    "v_cvt_pk_bf16_f32 v98, v92, v93\n"
    "v_cvt_pk_bf16_f32 v99, v94, v95\n"
    "v_mfma_f32_16x16x32_bf16 v[156:159], a[144:147], v[136:139], v[156:159]\n"
    "s_add_u32 s9, s42, 655360\n"
    "v_mfma_f32_16x16x32_bf16 v[184:187], a[148:151], v[136:139], v[184:187]\n"
    "s_waitcnt vmcnt(13)\n"
    "buffer_store_dwordx4 v[96:99], v168, s[56:59], s9 offen\n"
    "v_mfma_f32_16x16x32_bf16 v[144:147], a[152:155], v[140:143], v[144:147]\n"
    "v_mfma_f32_16x16x32_bf16 v[148:151], a[156:159], v[140:143], v[148:151]\n"
    "v_mfma_f32_16x16x32_bf16 v[152:155], a[160:163], v[140:143], v[152:155]\n"
    "v_mfma_f32_16x16x32_bf16 v[156:159], a[164:167], v[140:143], v[156:159]\n"
    "v_mfma_f32_16x16x32_bf16 v[184:187], a[168:171], v[140:143], v[184:187]\n"
    "s_waitcnt vmcnt(12)\n"
    "v_mfma_f32_16x16x32_bf16 v[188:191], v[196:199], a[64:67], 0\n"
    "v_mfma_f32_16x16x32_bf16 v[192:195], v[200:203], a[64:67], 0\n"
    "v_mfma_f32_16x16x32_bf16 v[12:15], v[204:207], a[64:67], 0\n"
    "v_mfma_f32_16x16x32_bf16 v[16:19], v[208:211], a[64:67], 0\n"
    "v_mfma_f32_16x16x32_bf16 v[20:23], v[212:215], a[64:67], 0\n"
    "s_waitcnt vmcnt(11)\n"
    "v_mfma_f32_16x16x32_bf16 v[188:191], v[216:219], a[68:71], v[188:191]\n"
    "v_mfma_f32_16x16x32_bf16 v[192:195], v[220:223], a[68:71], v[192:195]\n"
    "ds_write_b128 v169, v[144:147] offset:0\n"
    "v_mfma_f32_16x16x32_bf16 v[12:15], v[224:227], a[68:71], v[12:15]\n"
    "v_mfma_f32_16x16x32_bf16 v[16:19], v[228:231], a[68:71], v[16:19]\n"
    "v_mfma_f32_16x16x32_bf16 v[20:23], v[232:235], a[68:71], v[20:23]\n"
    "s_waitcnt vmcnt(10)\n"
    "ds_write_b128 v169, v[148:151] offset:1024\n"
    "v_mfma_f32_16x16x32_bf16 v[188:191], v[236:239], a[72:75], v[188:191]\n"
    "v_mfma_f32_16x16x32_bf16 v[192:195], v[240:243], a[72:75], v[192:195]\n"
    "v_mfma_f32_16x16x32_bf16 v[12:15], v[244:247], a[72:75], v[12:15]\n"
    "ds_write_b128 v169, v[152:155] offset:2048\n"
    "v_mfma_f32_16x16x32_bf16 v[16:19], v[248:251], a[72:75], v[16:19]\n"
    "v_mfma_f32_16x16x32_bf16 v[20:23], v[252:255], a[72:75], v[20:23]\n"
    "s_waitcnt vmcnt(9)\n"
    "v_mfma_f32_16x16x32_bf16 v[188:191], a[240:243], a[76:79], v[188:191]\n"
    "ds_write_b128 v169, v[156:159] offset:3072\n"
    "v_mfma_f32_16x16x32_bf16 v[192:195], a[244:247], a[76:79], v[192:195]\n"
    "v_mfma_f32_16x16x32_bf16 v[12:15], a[248:251], a[76:79], v[12:15]\n"
    "v_mfma_f32_16x16x32_bf16 v[16:19], a[252:255], a[76:79], v[16:19]\n"
    "ds_write_b128 v169, v[184:187] offset:4096\n"
    "v_mfma_f32_16x16x32_bf16 v[20:23], v[24:27], a[76:79], v[20:23]\n"
    "s_waitcnt vmcnt(8)\n"
    "v_mfma_f32_16x16x32_bf16 v[188:191], v[28:31], a[80:83], v[188:191]\n"
    "s_waitcnt lgkmcnt(0)\n"
    "s_barrier\n"
    "v_mfma_f32_16x16x32_bf16 v[192:195], v[32:35], a[80:83], v[192:195]\n"
    "ds_read_b128 v[88:91], v166 offset:0\n"
    "v_mfma_f32_16x16x32_bf16 v[12:15], v[36:39], a[80:83], v[12:15]\n"
    "ds_read_b128 v[92:95], v166 offset:256\n"
    "v_mfma_f32_16x16x32_bf16 v[16:19], v[40:43], a[80:83], v[16:19]\n"
    "ds_read_b128 v[96:99], v166 offset:5120\n"
    "v_mfma_f32_16x16x32_bf16 v[20:23], v[44:47], a[80:83], v[20:23]\n"
    "s_waitcnt vmcnt(7)\n"
    "ds_read_b128 v[100:103], v166 offset:5376\n"
    "v_mfma_f32_16x16x32_bf16 v[188:191], v[48:51], a[84:87], v[188:191]\n"
    "ds_read_b128 v[104:107], v166 offset:10240\n"
    "v_mfma_f32_16x16x32_bf16 v[192:195], v[52:55], a[84:87], v[192:195]\n"
    "ds_read_b128 v[108:111], v166 offset:10496\n"
    "v_mfma_f32_16x16x32_bf16 v[12:15], v[56:59], a[84:87], v[12:15]\n"
    "ds_read_b128 v[112:115], v166 offset:15360\n"
    "v_mfma_f32_16x16x32_bf16 v[16:19], v[60:63], a[84:87], v[16:19]\n"
    "ds_read_b128 v[116:119], v166 offset:15616\n"
    "v_mfma_f32_16x16x32_bf16 v[20:23], a[0:3], a[84:87], v[20:23]\n"
    "s_waitcnt vmcnt(6)\n"
    "v_mfma_f32_16x16x32_bf16 v[188:191], a[4:7], a[88:91], v[188:191]\n"
    "v_mfma_f32_16x16x32_bf16 v[192:195], a[8:11], a[88:91], v[192:195]\n"
    "s_waitcnt lgkmcnt(5)\n"
    "v_add_f32 v88, v88, v96\n"
    "v_add_f32 v89, v89, v97\n"
    "v_mfma_f32_16x16x32_bf16 v[12:15], a[12:15], a[88:91], v[12:15]\n"
    "v_add_f32 v90, v90, v98\n"
    "v_add_f32 v91, v91, v99\n"
    "v_mfma_f32_16x16x32_bf16 v[16:19], a[16:19], a[88:91], v[16:19]\n"
    "s_waitcnt lgkmcnt(4)\n"
    "v_add_f32 v92, v92, v100\n"
    "v_add_f32 v93, v93, v101\n"
    "v_mfma_f32_16x16x32_bf16 v[20:23], a[20:23], a[88:91], v[20:23]\n"
    "s_waitcnt vmcnt(5)\n"
    "v_add_f32 v94, v94, v102\n"
    "v_add_f32 v95, v95, v103\n"
    "v_mfma_f32_16x16x32_bf16 v[188:191], a[24:27], a[92:95], v[188:191]\n"
    "s_waitcnt lgkmcnt(1)\n"
    "v_add_f32 v104, v104, v112\n"
    "v_add_f32 v105, v105, v113\n"
    "v_mfma_f32_16x16x32_bf16 v[192:195], a[28:31], a[92:95], v[192:195]\n"
    "v_add_f32 v106, v106, v114\n"
    "v_add_f32 v107, v107, v115\n"
    "v_mfma_f32_16x16x32_bf16 v[12:15], a[32:35], a[92:95], v[12:15]\n"
    "s_waitcnt lgkmcnt(0)\n"
    "v_add_f32 v108, v108, v116\n"
    "v_add_f32 v109, v109, v117\n"
    "v_mfma_f32_16x16x32_bf16 v[16:19], a[36:39], a[92:95], v[16:19]\n"
    "v_add_f32 v110, v110, v118\n"
    "v_add_f32 v111, v111, v119\n"
    "v_mfma_f32_16x16x32_bf16 v[20:23], a[40:43], a[92:95], v[20:23]\n"
    "s_waitcnt vmcnt(4)\n"
    "v_lshlrev_b32 v96, 16, v172\n"
    "v_and_b32 v97, 0xffff0000, v172\n"
    "v_mfma_f32_16x16x32_bf16 v[188:191], a[44:47], a[96:99], v[188:191]\n"
    "v_lshlrev_b32 v98, 16, v173\n"
    "v_and_b32 v99, 0xffff0000, v173\n"
    "v_mfma_f32_16x16x32_bf16 v[192:195], a[48:51], a[96:99], v[192:195]\n"
    "v_lshlrev_b32 v100, 16, v174\n"
    "v_and_b32 v101, 0xffff0000, v174\n"
    "v_mfma_f32_16x16x32_bf16 v[12:15], a[52:55], a[96:99], v[12:15]\n"
    "v_lshlrev_b32 v102, 16, v175\n"
    "v_and_b32 v103, 0xffff0000, v175\n"
    "v_mfma_f32_16x16x32_bf16 v[16:19], a[56:59], a[96:99], v[16:19]\n"
    "v_add_f32 v88, v88, v104\n"
    "v_add_f32 v89, v89, v105\n"
    "v_mfma_f32_16x16x32_bf16 v[20:23], a[60:63], a[96:99], v[20:23]\n"
    "s_waitcnt vmcnt(3)\n"
    "v_add_f32 v90, v90, v106\n"
    "v_add_f32 v91, v91, v107\n"
    "v_mfma_f32_16x16x32_bf16 v[188:191], a[112:115], a[100:103], v[188:191]\n"
    "v_add_f32 v92, v92, v108\n"
    "v_add_f32 v93, v93, v109\n"
    "v_mfma_f32_16x16x32_bf16 v[192:195], a[116:119], a[100:103], v[192:195]\n"
    "v_add_f32 v94, v94, v110\n"
    "v_add_f32 v95, v95, v111\n"
    "v_mfma_f32_16x16x32_bf16 v[12:15], a[120:123], a[100:103], v[12:15]\n"
    "v_add_f32 v88, v88, v96\n"
    "v_add_f32 v89, v89, v97\n"
    "v_mfma_f32_16x16x32_bf16 v[16:19], a[124:127], a[100:103], v[16:19]\n"
    "v_add_f32 v90, v90, v98\n"
    "v_add_f32 v91, v91, v99\n"
    "v_mfma_f32_16x16x32_bf16 v[20:23], a[128:131], a[100:103], v[20:23]\n"
    "s_waitcnt vmcnt(2)\n"
    "v_add_f32 v92, v92, v100\n"
    "v_add_f32 v93, v93, v101\n"
    "v_mfma_f32_16x16x32_bf16 v[188:191], a[132:135], a[104:107], v[188:191]\n"
    "v_add_f32 v94, v94, v102\n"
    "v_add_f32 v95, v95, v103\n"
    "v_mfma_f32_16x16x32_bf16 v[192:195], a[136:139], a[104:107], v[192:195]\n"
    "v_cvt_pk_bf16_f32 v96, v88, v89\n"
    "v_cvt_pk_bf16_f32 v97, v90, v91\n"
    "v_mfma_f32_16x16x32_bf16 v[12:15], a[140:143], a[104:107], v[12:15]\n"
    "v_cvt_pk_bf16_f32 v98, v92, v93\n"
    "v_cvt_pk_bf16_f32 v99, v94, v95\n"
    "v_mfma_f32_16x16x32_bf16 v[16:19], a[144:147], a[104:107], v[16:19]\n"
    "s_add_u32 s9, s42, 1310720\n"
    "v_mfma_f32_16x16x32_bf16 v[20:23], a[148:151], a[104:107], v[20:23]\n"
    "s_waitcnt vmcnt(1)\n"
    "buffer_store_dwordx4 v[96:99], v168, s[56:59], s9 offen\n"
    "v_mfma_f32_16x16x32_bf16 v[188:191], a[152:155], a[108:111], v[188:191]\n"
    "v_mfma_f32_16x16x32_bf16 v[192:195], a[156:159], a[108:111], v[192:195]\n"
    "v_mfma_f32_16x16x32_bf16 v[12:15], a[160:163], a[108:111], v[12:15]\n"
    "v_mfma_f32_16x16x32_bf16 v[16:19], a[164:167], a[108:111], v[16:19]\n"
    "v_mfma_f32_16x16x32_bf16 v[20:23], a[168:171], a[108:111], v[20:23]\n"
    "s_nop 3\n"
    "ds_write_b128 v169, v[188:191] offset:20480\n"
    "ds_write_b128 v169, v[192:195] offset:21504\n"
    "ds_write_b128 v169, v[12:15] offset:22528\n"
    "ds_write_b128 v169, v[16:19] offset:23552\n"
    "ds_write_b128 v169, v[20:23] offset:24576\n"
    "s_waitcnt lgkmcnt(0)\n"
    "s_barrier\n"
    "ds_read_b128 v[88:91], v166 offset:20480\n"
    "ds_read_b128 v[92:95], v166 offset:20736\n"
    "ds_read_b128 v[96:99], v166 offset:25600\n"
    "ds_read_b128 v[100:103], v166 offset:25856\n"
    "ds_read_b128 v[104:107], v166 offset:30720\n"
    "ds_read_b128 v[108:111], v166 offset:30976\n"
    "ds_read_b128 v[112:115], v166 offset:35840\n"
    "ds_read_b128 v[116:119], v166 offset:36096\n"
    "s_waitcnt lgkmcnt(5)\n"
    "v_pk_add_f32 v[88:89], v[88:89], v[96:97]\n"
    "v_pk_add_f32 v[90:91], v[90:91], v[98:99]\n"
    "s_waitcnt lgkmcnt(4)\n"
    "v_pk_add_f32 v[92:93], v[92:93], v[100:101]\n"
    "v_pk_add_f32 v[94:95], v[94:95], v[102:103]\n"
    "s_waitcnt lgkmcnt(1)\n"
    "v_pk_add_f32 v[104:105], v[104:105], v[112:113]\n"
    "v_pk_add_f32 v[106:107], v[106:107], v[114:115]\n"
    "s_waitcnt lgkmcnt(0)\n"
    "v_pk_add_f32 v[108:109], v[108:109], v[116:117]\n"
    "v_pk_add_f32 v[110:111], v[110:111], v[118:119]\n"
    "v_lshlrev_b32 v96, 16, v180\n"
    "v_and_b32 v97, 0xffff0000, v180\n"
    "v_lshlrev_b32 v98, 16, v181\n"
    "v_and_b32 v99, 0xffff0000, v181\n"
    "v_lshlrev_b32 v100, 16, v182\n"
    "v_and_b32 v101, 0xffff0000, v182\n"
    "v_lshlrev_b32 v102, 16, v183\n"
    "v_and_b32 v103, 0xffff0000, v183\n"
    "v_pk_add_f32 v[88:89], v[88:89], v[104:105]\n"
    "v_pk_add_f32 v[90:91], v[90:91], v[106:107]\n"
    "v_pk_add_f32 v[92:93], v[92:93], v[108:109]\n"
    "v_pk_add_f32 v[94:95], v[94:95], v[110:111]\n"
    "v_pk_add_f32 v[88:89], v[88:89], v[96:97]\n"
    "v_pk_add_f32 v[90:91], v[90:91], v[98:99]\n"
    "v_pk_add_f32 v[92:93], v[92:93], v[100:101]\n"
    "v_pk_add_f32 v[94:95], v[94:95], v[102:103]\n"
    "v_cvt_pk_bf16_f32 v96, v88, v89\n"
    "v_cvt_pk_bf16_f32 v97, v90, v91\n"
    "v_cvt_pk_bf16_f32 v98, v92, v93\n"
    "v_cvt_pk_bf16_f32 v99, v94, v95\n"
    "s_add_u32 s9, s42, 1966080\n"
    "buffer_store_dwordx4 v[96:99], v168, s[56:59], s9 offen\n"
    "s_or_b32 s8, s2, s3\n"
    "s_cbranch_scc1 .L42_%=\n"
    "v_mov_b32 v2, s64\n"
    "v_mov_b32 v3, s65\n"
    "s_mov_b64 exec, 1\n"
    "buffer_store_dwordx2 v[2:3], off, s[52:55], 0 sc1\n"
    "s_mov_b64 exec, -1\n"
    ".L42_%=:\n"
    ".END_%=:\n"
    :
    : "{s[0:1]}"(__builtin_amdgcn_kernarg_segment_ptr()), "{s2}"(blockIdx.x), "{v0}"(threadIdx.x + (uint32_t)(size_t)(__attribute__((address_space(3))) char*)smem)
    : "v1", "v2", "v3", "v4", "v5", "v6", "v7", "v8", "v9", "v10", "v11", "v12", "v13", "v14", "v15", "v16", "v17", "v18", "v19", "v20", "v21", "v22", "v23", "v24", "v25", "v26", "v27", "v28", "v29", "v30", "v31", "v32", "v33", "v34", "v35", "v36", "v37", "v38", "v39", "v40", "v41", "v42", "v43", "v44", "v45", "v46", "v47", "v48", "v49", "v50", "v51", "v52", "v53", "v54", "v55", "v56", "v57", "v58", "v59", "v60", "v61", "v62", "v63", "v64", "v65", "v66", "v67", "v68", "v69", "v70", "v71", "v72", "v73", "v74", "v75", "v76", "v77", "v78", "v79", "v80", "v81", "v82", "v83", "v84", "v85", "v86", "v87", "v88", "v89", "v90", "v91", "v92", "v93", "v94", "v95", "v96", "v97", "v98", "v99", "v100", "v101", "v102", "v103", "v104", "v105", "v106", "v107", "v108", "v109", "v110", "v111", "v112", "v113", "v114", "v115", "v116", "v117", "v118", "v119", "v120", "v121", "v122", "v123", "v124", "v125", "v126", "v127", "v128", "v129", "v130", "v131", "v132", "v133", "v134", "v135", "v136", "v137", "v138", "v139", "v140", "v141", "v142", "v143", "v144", "v145", "v146", "v147", "v148", "v149", "v150", "v151", "v152", "v153", "v154", "v155", "v156", "v157", "v158", "v159", "v160", "v161", "v162", "v163", "v164", "v165", "v166", "v167", "v168", "v169", "v170", "v171", "v172", "v173", "v174", "v175", "v176", "v177", "v178", "v179", "v180", "v181", "v182", "v183", "v184", "v185", "v186", "v187", "v188", "v189", "v190", "v191", "v192", "v193", "v194", "v195", "v196", "v197", "v198", "v199", "v200", "v201", "v202", "v203", "v204", "v205", "v206", "v207", "v208", "v209", "v210", "v211", "v212", "v213", "v214", "v215", "v216", "v217", "v218", "v219", "v220", "v221", "v222", "v223", "v224", "v225", "v226", "v227", "v228", "v229", "v230", "v231", "v232", "v233", "v234", "v235", "v236", "v237", "v238", "v239", "v240", "v241", "v242", "v243", "v244", "v245", "v246", "v247", "v248", "v249", "v250", "v251", "v252", "v253", "v254", "v255", "a0", "a1", "a2", "a3", "a4", "a5", "a6", "a7", "a8", "a9", "a10", "a11", "a12", "a13", "a14", "a15", "a16", "a17", "a18", "a19", "a20", "a21", "a22", "a23", "a24", "a25", "a26", "a27", "a28", "a29", "a30", "a31", "a32", "a33", "a34", "a35", "a36", "a37", "a38", "a39", "a40", "a41", "a42", "a43", "a44", "a45", "a46", "a47", "a48", "a49", "a50", "a51", "a52", "a53", "a54", "a55", "a56", "a57", "a58", "a59", "a60", "a61", "a62", "a63", "a64", "a65", "a66", "a67", "a68", "a69", "a70", "a71", "a72", "a73", "a74", "a75", "a76", "a77", "a78", "a79", "a80", "a81", "a82", "a83", "a84", "a85", "a86", "a87", "a88", "a89", "a90", "a91", "a92", "a93", "a94", "a95", "a96", "a97", "a98", "a99", "a100", "a101", "a102", "a103", "a104", "a105", "a106", "a107", "a108", "a109", "a110", "a111", "a112", "a113", "a114", "a115", "a116", "a117", "a118", "a119", "a120", "a121", "a122", "a123", "a124", "a125", "a126", "a127", "a128", "a129", "a130", "a131", "a132", "a133", "a134", "a135", "a136", "a137", "a138", "a139", "a140", "a141", "a142", "a143", "a144", "a145", "a146", "a147", "a148", "a149", "a150", "a151", "a152", "a153", "a154", "a155", "a156", "a157", "a158", "a159", "a160", "a161", "a162", "a163", "a164", "a165", "a166", "a167", "a168", "a169", "a170", "a171", "a172", "a173", "a174", "a175", "a176", "a177", "a178", "a179", "a180", "a181", "a182", "a183", "a184", "a185", "a186", "a187", "a188", "a189", "a190", "a191", "a192", "a193", "a194", "a195", "a196", "a197", "a198", "a199", "a200", "a201", "a202", "a203", "a204", "a205", "a206", "a207", "a208", "a209", "a210", "a211", "a212", "a213", "a214", "a215", "a216", "a217", "a218", "a219", "a220", "a221", "a222", "a223", "a224", "a225", "a226", "a227", "a228", "a229", "a230", "a231", "a232", "a233", "a234", "a235", "a236", "a237", "a238", "a239", "a240", "a241", "a242", "a243", "a244", "a245", "a246", "a247", "a248", "a249", "a250", "a251", "a252", "a253", "a254", "a255", "s3", "s4", "s5", "s6", "s7", "s8", "s9", "s10", "s11", "s12", "s13", "s14", "s15", "s16", "s17", "s18", "s19", "s20", "s21", "s22", "s23", "s24", "s25", "s26", "s27", "s28", "s29", "s30", "s31", "s33", "s34", "s35", "s36", "s37", "s38", "s39", "s40", "s41", "s42", "s43", "s44", "s45", "s46", "s47", "s48", "s49", "s50", "s51", "s52", "s53", "s54", "s55", "s56", "s57", "s58", "s59", "s60", "s61", "s62", "s63", "s64", "s65", "s66", "s67", "s68", "s69", "s70", "s71", "s72", "s73", "s74", "s75", "s76", "s77", "s78", "s79", "s80", "s81", "s82", "s83", "s84", "s85", "s86", "s87", "s88", "s89", "s90", "s91", "s92", "s93", "s94", "s95", "s96", "s97", "s98", "s99", "vcc", "scc", "memory");
}
}

extern "C" void kernel_launch(void* const* d_in, const int* in_sizes, int n_in, void* d_out, int out_size, void* d_ws, size_t ws_size,
                              hipStream_t stream) {
  constexpr uint32_t FL = 0x00020000u, SD = 2048u * 640u * 2u, DF = 640u * 1536u * 2u;
  Args a;
  a.d[0] = Desc{d_in[0], SD, FL};
  a.d[1] = Desc{d_in[1], SD, FL};
  a.d[2] = Desc{d_in[2], SD, FL};
  a.d[3] = Desc{d_in[3], 640u * 4u * 2u, FL};
  a.d[4] = Desc{d_in[4], SD, FL};
  a.d[5] = Desc{d_in[5], 640u * 2u, FL};
  a.d[6] = Desc{d_in[7], DF, FL};
  a.d[7] = Desc{d_in[8], DF, FL};
  a.d[8] = Desc{d_in[9], DF, FL};
  a.d[9] = Desc{d_ws, 21889024u, FL};
  a.d[10] = Desc{d_out, SD, FL};
  a.d[11] = Desc{d_in[7], DF, FL};
  k_block<<<256, 256, 0, stream>>>(a);
}
```
